```python
import torch

def _softmax(x, axis=-1):
    return torch.softmax(x, dim=axis)
def _gelu(x):
    return torch.nn.functional.gelu(x, approximate="tanh")
def _silu(x):
    return torch.nn.functional.silu(x)
def _sigmoid(x):
    return torch.sigmoid(x)
def _layernorm(x, axis=-1, eps=1e-5):
    m = x.mean(dim=axis, keepdim=True)
    v = x.var(dim=axis, keepdim=True, unbiased=False)
    return (x - m) / torch.sqrt(v + eps)
def _scatter_add(tgt, idx, src, axis=0):
    return tgt.clone().index_add_(axis, idx, src)
def _segment_sum(src, seg, n):
    out = torch.zeros((n,) + src.shape[1:], dtype=src.dtype)
    return out.index_add_(0, seg, src)
def _pad(x, pw):
    flat = tuple(v for pair in reversed(pw) for v in pair)
    return torch.nn.functional.pad(x, flat)

kernel_name = 'sequential_preLN__spectral_fnet__glu_gated_glu__prompt'

def setup_inputs():

    _w = (lambda a: (lambda t: t.to(torch.bfloat16) if t.is_floating_point() else t)(torch.as_tensor(a)))
    def _A_setup():
        B, S, D = 1, 2048, 640
        ns = torch.arange(S)[:, None] * torch.arange(S)[None, :]
        nd = torch.arange(D)[:, None] * torch.arange(D)[None, :]
        Ws_re = torch.cos(-2.0 * torch.pi * ns / S).to(float)
        Ws_im = torch.sin(-2.0 * torch.pi * ns / S).to(float)
        Wd_re = torch.cos(-2.0 * torch.pi * nd / D).to(float)
        Wd_im = torch.sin(-2.0 * torch.pi * nd / D).to(float)
        return {
            "x": _w((lambda *s: torch.randn(*s, dtype=torch.bfloat16) * 0.05)(B, S, D)),
            "dft_seq_re": _w(Ws_re),
            "dft_seq_im": _w(Ws_im),
            "dft_hid_re": _w(Wd_re),
            "dft_hid_im": _w(Wd_im),
        }
    _A_in = _A_setup()

    def _B_setup():
        B, S, D, F = 1, 2048, 640, 1728
        return {
            "x": _w((lambda *s: torch.randn(*s, dtype=torch.bfloat16) * 0.05)(B, S, D)),
            "w_gate": _w((lambda *s: torch.randn(*s, dtype=torch.bfloat16) * 1.6)(D, F)),
            "w_up": _w((lambda *s: torch.randn(*s, dtype=torch.bfloat16) * 0.8)(D, F)),
            "w_down": _w((lambda *s: torch.randn(*s, dtype=torch.bfloat16) * 0.8)(F, D)),
        }
    _B_in = _B_setup()

    return {
        "A_x": _A_in["x"],
        "A_dft_seq_re": _A_in["dft_seq_re"],
        "A_dft_seq_im": _A_in["dft_seq_im"],
        "A_dft_hid_re": _A_in["dft_hid_re"],
        "A_dft_hid_im": _A_in["dft_hid_im"],
        "B_x": _B_in["x"],
        "B_w_gate": _B_in["w_gate"],
        "B_w_up": _B_in["w_up"],
        "B_w_down": _B_in["w_down"],
    }

def reference(A_x, A_dft_seq_re, A_dft_seq_im, A_dft_hid_re, A_dft_hid_im, B_x, B_w_gate, B_w_up, B_w_down):
    def _A_ref(x, dft_seq_re, dft_seq_im, dft_hid_re, dft_hid_im):
        a = torch.einsum("bsd,ed->bse", x, dft_hid_re)
        b = torch.einsum("bsd,ed->bse", x, dft_hid_im)
        y = torch.einsum("ts,bse->bte", dft_seq_re, a) - torch.einsum("ts,bse->bte", dft_seq_im, b)
        return y

    def _B_ref(x, w_gate, w_up, w_down):
        g = torch.einsum("bsd,df->bsf", x, w_gate)
        u = torch.einsum("bsd,df->bsf", x, w_up)
        h = _sigmoid(g) * u
        return torch.einsum("bsf,fd->bsd", h, w_down)

    x = A_x
    h = x + _A_ref(_layernorm(x, axis=-1), A_dft_seq_re, A_dft_seq_im, A_dft_hid_re, A_dft_hid_im)
    h2 = h + _B_ref(_layernorm(h, axis=-1), B_w_gate, B_w_up, B_w_down)
    return h2
```

```cpp
#include <hip/hip_runtime.h>
#include <hip/hip_bf16.h>
typedef unsigned short bf16_t;
typedef short bf16x8 __attribute__((ext_vector_type(8)));
typedef float f32x4 __attribute__((ext_vector_type(4)));

__device__ __forceinline__ float bf2f(bf16_t v) { return __uint_as_float(((unsigned)v) << 16); }
__device__ __forceinline__ bf16_t f2bf(float f) { __hip_bfloat16 h = __float2bfloat16(f); return *(bf16_t*)&h; }

constexpr int S = 2048, D = 640, F = 1728;

template <typename T>
__global__ void ln_kernel(const T* __restrict__ in, bf16_t* __restrict__ out) {
  int row = blockIdx.x; int tid = threadIdx.x;
  float v[10]; float s = 0.f;
  for (int i = 0; i < 10; ++i) { float x; if constexpr (sizeof(T) == 2) x = bf2f(((const bf16_t*)in)[row * D + i * 64 + tid]); else x = ((const float*)in)[row * D + i * 64 + tid]; v[i] = x; s += x; }
  for (int o = 32; o > 0; o >>= 1) s += __shfl_xor(s, o);
  float m = s / D; float q = 0.f;
  for (int i = 0; i < 10; ++i) { float d = v[i] - m; q += d * d; }
  for (int o = 32; o > 0; o >>= 1) q += __shfl_xor(q, o);
  float r = 1.0f / sqrtf(q / D + 1e-5f);
  for (int i = 0; i < 10; ++i) out[row * D + i * 64 + tid] = f2bf((v[i] - m) * r);
}

template <bool BT, int EPI>
__global__ void gemm_naive(const bf16_t* __restrict__ A, int lda, const bf16_t* __restrict__ B, const bf16_t* __restrict__ B2, int ldb, int K,
                           void* __restrict__ C, int ldc, const void* __restrict__ aux) {
  int lane = threadIdx.x & 63; int m0 = blockIdx.y * 16, n0 = blockIdx.x * 16;
  int r = lane & 15, q = lane >> 4;
  f32x4 acc = {0, 0, 0, 0}, acc2 = {0, 0, 0, 0};
  for (int k0 = 0; k0 < K; k0 += 32) {
    bf16x8 a = *(const bf16x8*)(A + (size_t)(m0 + r) * lda + k0 + q * 8);
    bf16x8 b, b2;
    if (BT) { b = *(const bf16x8*)(B + (size_t)(n0 + r) * ldb + k0 + q * 8); if (EPI == 2) b2 = *(const bf16x8*)(B2 + (size_t)(n0 + r) * ldb + k0 + q * 8); }
    else { for (int j = 0; j < 8; ++j) { b[j] = B[(size_t)(k0 + q * 8 + j) * ldb + n0 + r]; if (EPI == 2) b2[j] = B2[(size_t)(k0 + q * 8 + j) * ldb + n0 + r]; } }
    acc = __builtin_amdgcn_mfma_f32_16x16x32_bf16(a, b, acc, 0, 0, 0);
    if (EPI == 2) acc2 = __builtin_amdgcn_mfma_f32_16x16x32_bf16(a, b2, acc2, 0, 0, 0);
  }
  for (int j = 0; j < 4; ++j) {
    int row = m0 + q * 4 + j, col = n0 + r; size_t idx = (size_t)row * ldc + col;
    if (EPI == 0) ((bf16_t*)C)[idx] = f2bf(acc[j]);
    if (EPI == 1) ((float*)C)[idx] = acc[j] + bf2f(((const bf16_t*)aux)[idx]);
    if (EPI == 3) ((float*)C)[idx] -= acc[j];
    if (EPI == 2) { float g = acc[j], u = acc2[j]; ((bf16_t*)C)[idx] = f2bf(u / (1.f + __expf(-g))); }
    if (EPI == 4) ((bf16_t*)C)[idx] = f2bf(acc[j] + ((const float*)aux)[idx]);
  }
}

extern "C" void kernel_launch(void* const* d_in, const int* in_sizes, int n_in, void* d_out, int out_size, void* d_ws, size_t ws_size, hipStream_t stream) {
  const bf16_t* x = (const bf16_t*)d_in[0];
  const bf16_t* Cre = (const bf16_t*)d_in[1]; const bf16_t* Cim = (const bf16_t*)d_in[2];
  const bf16_t* Wre = (const bf16_t*)d_in[3]; const bf16_t* Wim = (const bf16_t*)d_in[4];
  const bf16_t* Wg = (const bf16_t*)d_in[6]; const bf16_t* Wu = (const bf16_t*)d_in[7]; const bf16_t* Wd = (const bf16_t*)d_in[8];
  char* ws = (char*)d_ws;
  bf16_t* xn = (bf16_t*)(ws); bf16_t* a = (bf16_t*)(ws + (4 << 20)); bf16_t* b = (bf16_t*)(ws + (8 << 20));
  float* h = (float*)(ws + (12 << 20)); bf16_t* hn = (bf16_t*)(ws + (20 << 20)); bf16_t* hh = (bf16_t*)(ws + (24 << 20));
  ln_kernel<bf16_t><<<S, 64, 0, stream>>>(x, xn);
  gemm_naive<true, 0><<<dim3(D / 16, S / 16), 64, 0, stream>>>(xn, D, Wre, nullptr, D, D, a, D, nullptr);
  gemm_naive<true, 0><<<dim3(D / 16, S / 16), 64, 0, stream>>>(xn, D, Wim, nullptr, D, D, b, D, nullptr);
  gemm_naive<false, 1><<<dim3(D / 16, S / 16), 64, 0, stream>>>(Cre, S, a, nullptr, D, S, h, D, x);
  gemm_naive<false, 3><<<dim3(D / 16, S / 16), 64, 0, stream>>>(Cim, S, b, nullptr, D, S, h, D, nullptr);
  ln_kernel<float><<<S, 64, 0, stream>>>(h, hn);
  gemm_naive<false, 2><<<dim3(F / 16, S / 16), 64, 0, stream>>>(hn, D, Wg, Wu, F, D, hh, F, nullptr);
  gemm_naive<false, 4><<<dim3(D / 16, S / 16), 64, 0, stream>>>(hh, F, Wd, nullptr, D, F, d_out, D, h);
}
```

```cpp
#include <hip/hip_runtime.h>
#include <hip/hip_bf16.h>
typedef unsigned short bf16_t;
typedef short bf16x8 __attribute__((ext_vector_type(8)));
typedef float f32x4 __attribute__((ext_vector_type(4)));
typedef float f32x2 __attribute__((ext_vector_type(2)));
typedef unsigned u32x4 __attribute__((ext_vector_type(4)));
typedef unsigned u32x2 __attribute__((ext_vector_type(2)));
typedef __bf16 bf16x2_t __attribute__((ext_vector_type(2)));
#define LAS __attribute__((address_space(3)))
#define GAS __attribute__((address_space(1)))
#define DEV __device__ __forceinline__

constexpr int S = 2048, D = 640, F = 1728;
constexpr unsigned WS_ABF = 0;
constexpr unsigned WS_HF = WS_ABF + 5242880;
constexpr unsigned WS_WF = WS_HF + 2621440;
constexpr unsigned WS_HHF = WS_WF + 4423680;
constexpr unsigned WS_WDF = WS_HHF + 7077888;
constexpr unsigned WS_XB = WS_WDF + 2211840;
constexpr unsigned WS_CS = WS_XB + 5242880;
constexpr unsigned WS_ST = WS_CS + 138240;
constexpr unsigned WS_FL = WS_ST + 131072;
constexpr unsigned WS_END = WS_FL + 65536;
#define TOUCH 0
#define ROT 2
#define ROT1 1
#define ROT3 0
#define ST_AUX 16

template <int AUX = 0> DEV void glds16(const void* g, LAS void* l) {
  __builtin_amdgcn_global_load_lds((const GAS unsigned*)g, (LAS unsigned*)l, 16, 0, AUX);
}
DEV void glds4(const void* g, LAS void* l) { __builtin_amdgcn_global_load_lds((const GAS unsigned*)g, (LAS unsigned*)l, 4, 0, 0); }
template <int N> DEV void wait_v() { asm volatile("s_waitcnt vmcnt(%0)" ::"i"(N) : "memory"); }
DEV void wait_l0() { asm volatile("s_waitcnt lgkmcnt(0)" ::: "memory"); }
DEV void bar() { asm volatile("" ::: "memory"); __builtin_amdgcn_s_barrier(); asm volatile("" ::: "memory"); }
DEV f32x4 mfma(bf16x8 a, bf16x8 b, f32x4 c) { return __builtin_amdgcn_mfma_f32_16x16x32_bf16(a, b, c, 0, 0, 0); }
DEV unsigned pk2(float lo, float hi) { bf16x2_t v; v.x = (__bf16)lo; v.y = (__bf16)hi; return __builtin_bit_cast(unsigned, v); }
DEV float bflo(unsigned u) { return __uint_as_float(u << 16); }
DEV float bfhi(unsigned u) { return __uint_as_float(u & 0xffff0000u); }
DEV float dot2(unsigned a, unsigned b, float c) { return __builtin_amdgcn_fdot2_f32_bf16(__builtin_bit_cast(bf16x2_t, a), __builtin_bit_cast(bf16x2_t, b), c, false); }
DEV __amdgpu_buffer_rsrc_t mkrsrc(const void* p, unsigned bytes) { return __builtin_amdgcn_make_buffer_rsrc((void*)p, 0, bytes, 0x00020000); }
DEV void st16(__amdgpu_buffer_rsrc_t r, unsigned off, u32x4 v) { __builtin_amdgcn_raw_buffer_store_b128(v, r, off, 0, ST_AUX); }
DEV void st8(__amdgpu_buffer_rsrc_t r, unsigned off, u32x2 v) { __builtin_amdgcn_raw_buffer_store_b64(v, r, off, 0, ST_AUX); }
DEV void st4(__amdgpu_buffer_rsrc_t r, unsigned off, unsigned v) { __builtin_amdgcn_raw_buffer_store_b32(v, r, off, 0, ST_AUX); }
DEV float qsum(float v) {
  auto a = __builtin_amdgcn_permlane16_swap(__float_as_uint(v), __float_as_uint(v), false, false);
  v = __uint_as_float(a[0]) + __uint_as_float(a[1]);
  auto b = __builtin_amdgcn_permlane32_swap(__float_as_uint(v), __float_as_uint(v), false, false);
  return __uint_as_float(b[0]) + __uint_as_float(b[1]);
}

DEV u32x4 pairfrag(unsigned a0, unsigned a1, unsigned b0, unsigned b1) {
  auto p0 = __builtin_amdgcn_permlane32_swap(a0, b0, false, false);
  auto p1 = __builtin_amdgcn_permlane32_swap(a1, b1, false, false);
  auto q0 = __builtin_amdgcn_permlane16_swap(p0[0], p0[1], false, false);
  auto q1 = __builtin_amdgcn_permlane16_swap(p1[0], p1[1], false, false);
  return (u32x4){q0[0], q1[0], q0[1], q1[1]};
}
#define STAMP(k, i)
#define STAMPL(k, i)
#define PWAIT()

template <int NS, int PW, class Issue, class Post>
DEV void loader_loop(Issue issue, Post post) {
  issue(0, 0); issue(1, 1);
  wait_v<PW>(); bar();
  issue(2, 2);
  post(0, 0);
  int slot = 0;
#pragma unroll 1
  for (int s = 0; s < NS - 1; ++s) {
    if (s + 2 < NS) wait_v<PW>(); else wait_v<0>();
    bar();
    if (s + 3 < NS) issue(s + 3, slot);
    slot = slot == 2 ? 0 : slot + 1;
    post(s + 1, slot);
  }
}
template <int NRD, int NMF> DEV void interleave() {
#pragma unroll
  for (int i = 0; i < NRD; ++i) { __builtin_amdgcn_sched_group_barrier(0x8, 1, 0); __builtin_amdgcn_sched_group_barrier(0x100, 1, 0); }
  __builtin_amdgcn_sched_group_barrier(0x8, NMF - NRD, 0);
}
#define SB0() __builtin_amdgcn_sched_barrier(0)
template <int NS, int KS, int KSL  , int NRD, int NMF, class Rd, class Cp>
DEV void consumer_loop(Rd rd, Cp cp) {
  rd(0, 0, 0);
  SB0();
  int slot = 0;
#pragma unroll 1
  for (int s = 0; s < NS - 1; ++s) {
    const int nslot = slot == 2 ? 0 : slot + 1;
#pragma unroll
    for (int ks = 0; ks < KS - 1; ++ks) { rd(slot, ks + 1, (ks + 1) & 1); cp(ks & 1); interleave<NRD, NMF>(); SB0(); }
    wait_l0(); bar();
    rd(nslot, 0, 0); cp(1); interleave<NRD, NMF>(); SB0();
    slot = nslot;
  }
#pragma unroll
  for (int ks = 0; ks < KSL - 1; ++ks) { rd(slot, ks + 1, (ks + 1) & 1); cp(ks & 1); interleave<NRD, NMF>(); SB0(); }
  cp((KSL - 1) & 1);
  SB0();
}

constexpr int ST128 = 52 * 1024;
__global__ __launch_bounds__(512, 2) void k1_kernel(const bf16_t* __restrict__ x, const bf16_t* __restrict__ Wre, const bf16_t* __restrict__ Wim,
                                                    const bf16_t* __restrict__ Cre, const bf16_t* __restrict__ Cim, const bf16_t* __restrict__ Wg,
                                                    const bf16_t* __restrict__ Wu, const bf16_t* __restrict__ Wd, char* __restrict__ ws) {
  extern __shared__ __attribute__((aligned(16))) char smem_[];
  LAS char* smem = (LAS char*)smem_;
  STAMP(0, 0);
  const int tid = threadIdx.x, lane = tid & 63, w8 = __builtin_amdgcn_readfirstlane(tid >> 6), w = w8 & 3;
  const int bid = blockIdx.x, xcd = bid & 7, idx = bid >> 3;
  const int half = xcd >> 2, te = idx & 7, ts = (xcd & 3) * 4 + (idx >> 3), g = half * 8 + te;
  const int fr = lane & 15, fq = lane >> 4;
  const int r8 = lane >> 3, c8 = lane & 7;
  const int rd0 = fr * 128 + ((fq ^ (fr >> 1)) << 4);
  const int rd1 = fr * 128 + (((fq + 4) ^ (fr >> 1)) << 4);
  LAS float* stat = (LAS float*)(smem + 3 * ST128);
  __amdgpu_buffer_rsrc_t wr = mkrsrc(ws, WS_END);
  if (w8 >= 4) {
    const int rowE = 8 * (r8 >> 2) + (r8 & 3);
    const bf16_t* xa = x + (size_t)(ts * 128 + w * 32) * D;
    const int sE = (c8 ^ (r8 >> 1)) << 3, sO = (c8 ^ (4 + (r8 >> 1))) << 3;
    const bf16_t* Abase = xa + rowE * D + sE;
    const bf16_t* AbaseO = xa + (rowE + 16) * D + sO;
    const bf16_t* wb = (half ? Wim : Wre) + (size_t)(te * 80 + (w & 1) * 40 + r8) * D + (w >> 1) * 64;
    const bf16_t* Bbase = wb + ((w & 1) ? sO : sE);
    const bf16_t* BbaseO = wb + ((w & 1) ? sE : sO);
    const int blk = bid * 4 + w;
    LAS char* tb = smem + 2 * ST128 + w * 8192;
    const int mat = blk / 270, rem = blk % 270;
    const bool tvalid = blk < 810;
    int kb, nb, ldw; const bf16_t* wsrc;
    if (mat < 2) { kb = rem / 27; nb = rem % 27; ldw = F; wsrc = mat ? Wu : Wg; } else { kb = rem / 10; nb = rem % 10; ldw = D; wsrc = Wd; }
    if (!tvalid) { kb = 0; nb = 0; }
    const int rot1 = ROT1 ? (te + 2 * (idx >> 3)) % 5 : 0;
    auto issue = [&](int s, int slot) {
      LAS char* d = smem + slot * ST128;
      s += rot1; if (s >= 5) s -= 5;
#pragma unroll
      for (int sub = 0; sub < 2; ++sub)
#pragma unroll
        for (int p = 0; p < 4; ++p) glds16(((p & 1) ? AbaseO : Abase) + (p >> 1) * 4 * D + s * 128 + sub * 64, d + sub * 16384 + (w * 4 + p) * 1024);
#pragma unroll
      for (int p = 0; p < 5; ++p) glds16(((p & 1) ? BbaseO : Bbase) + p * 8 * D + s * 128, d + 32768 + (w * 5 + p) * 1024);
    };
    float wsum[5] = {0.f, 0.f, 0.f, 0.f, 0.f};
    auto post = [&](int s, int slot) {
      LAS char* d = smem + slot * ST128;
#pragma unroll
      for (int n = 0; n < 5; ++n) {
        u32x4 u = *(LAS u32x4*)(d + 32768 + (w >> 1) * 10240 + n * 2048 + ((w & 1) ? rd1 : rd0));
#pragma unroll
        for (int i = 0; i < 4; ++i) wsum[n] = dot2(u[i], 0x3F803F80u, wsum[n]);
      }
      asm volatile("s_waitcnt lgkmcnt(0)" : "+v"(wsum[0]), "+v"(wsum[1]), "+v"(wsum[2]), "+v"(wsum[3]), "+v"(wsum[4]) :: "memory");
    };
    auto touch = [&]() {
      const bf16_t* M = half ? Cim : Cre;
      LAS char* dummy = smem + 3 * ST128 + 2560 + w * 256;
      const int t4 = tid & 255;
#pragma unroll
      for (int rep = 0; rep < 2; ++rep) {
        int row = (xcd & 3) * 512 + (idx * 2 + rep) * 8 + (t4 >> 5);
        glds4(M + (size_t)row * S + (t4 & 31) * 64, dummy);
      }
    };
    issue(0, 0); issue(1, 1);
    wait_v<13>(); bar();
    issue(2, 2); post(0, 0);
    wait_v<13>(); bar();
    issue(3, 0); post(1, 1);
    wait_v<13>(); bar();
    issue(4, 1); post(2, 2);
    wait_v<13>(); bar();
    {
      const bf16_t* src = wsrc + (size_t)(kb * 64 + r8) * ldw + nb * 64 + c8 * 8;
#pragma unroll
      for (int p = 0; p < 8; ++p) glds16(src + (size_t)p * 8 * ldw, tb + p * 1024);
    }
    post(3, 0);
    wait_v<8>(); bar();
    post(4, 1);
#pragma unroll
    for (int n = 0; n < 5; ++n) { const float v = qsum(wsum[n]); if (lane < 16) stat[256 + w * 80 + n * 16 + lane] = v; }
    wait_l0(); bar();
    wait_v<0>();
    if (lane == 0) st4(wr, WS_FL + (unsigned)(bid * 4 + w) * 64, 0u);
    STAMPL(0, 9);
    if (tvalid) {
      const unsigned tbase = (unsigned)(size_t)tb + ((fq * 8 + (fr >> 2)) * 128 + (fr & 3) * 8);
      u32x2 lo[4][2], hi[4][2];
#pragma unroll
      for (int c = 0; c < 4; ++c)
#pragma unroll
        for (int kk = 0; kk < 2; ++kk)
          asm volatile("ds_read_b64_tr_b16 %0, %2 offset:%3\n\tds_read_b64_tr_b16 %1, %2 offset:%4"
                       : "=&v"(lo[c][kk]), "=&v"(hi[c][kk]) : "v"(tbase), "i"(kk * 4096 + c * 32), "i"(kk * 4096 + 512 + c * 32) : "memory");
      asm volatile("s_waitcnt lgkmcnt(0)" : "+v"(lo[0][0]), "+v"(lo[0][1]), "+v"(lo[1][0]), "+v"(lo[1][1]), "+v"(lo[2][0]), "+v"(lo[2][1]), "+v"(lo[3][0]), "+v"(lo[3][1]),
                   "+v"(hi[0][0]), "+v"(hi[0][1]), "+v"(hi[1][0]), "+v"(hi[1][1]), "+v"(hi[2][0]), "+v"(hi[2][1]), "+v"(hi[3][0]), "+v"(hi[3][1]) :: "memory");
#pragma unroll
      for (int c = 0; c < 4; ++c) {
        float cs = 0.f;
#pragma unroll
        for (int kk = 0; kk < 2; ++kk) {
          u32x4 o = {lo[c][kk].x, lo[c][kk].y, hi[c][kk].x, hi[c][kk].y};
          unsigned dst;
          if (mat < 2) dst = WS_WF + (unsigned)(((kb * 2 + kk) * 108 + nb * 4 + c) * 2 + mat) * 1024;
          else { int eps = nb * 4 + c; dst = WS_WDF + (unsigned)(((eps / 5) * 54 + kb * 2 + kk) * 5 + eps % 5) * 1024; }
          st16(wr, dst + lane * 16, o);
#pragma unroll
          for (int i = 0; i < 4; ++i) cs = dot2(o[i], 0x3F803F80u, cs);
        }
        cs = qsum(cs);
        if (mat < 2 && lane < 16) st4(wr, WS_CS + (unsigned)((kb * 2 + mat) * F + nb * 64 + c * 16 + lane) * 4, __float_as_uint(cs));
      }
    }
    STAMPL(0, 10); PWAIT(); STAMPL(0, 11);
    return;
  }
  f32x4 acc[2][5];
#pragma unroll
  for (int m = 0; m < 2; ++m)
#pragma unroll
    for (int n = 0; n < 5; ++n) acc[m][n] = (f32x4){0, 0, 0, 0};
  bf16x8 fa[2][2], fb[2][5], ones;
  f32x4 xs[2] = {(f32x4){0, 0, 0, 0}, (f32x4){0, 0, 0, 0}}, xq[2] = {(f32x4){0, 0, 0, 0}, (f32x4){0, 0, 0, 0}};
#pragma unroll
  for (int i = 0; i < 8; ++i) ones[i] = 0x3F80;
  auto rd = [&](int slot, int ks, int set) {
    LAS char* d = smem + slot * ST128;
    const int r = (ks & 1) ? rd1 : rd0;
#pragma unroll
    for (int m = 0; m < 2; ++m) fa[set][m] = *(LAS bf16x8*)(d + (ks >> 1) * 16384 + (w * 2 + m) * 2048 + r);
#pragma unroll
    for (int n = 0; n < 5; ++n) fb[set][n] = *(LAS bf16x8*)(d + 32768 + (ks >> 1) * 10240 + n * 2048 + r);
  };
  auto cp = [&](int set) {
#pragma unroll
    for (int n = 0; n < 5; ++n)
#pragma unroll
      for (int m = 0; m < 2; ++m) acc[m][n] = mfma(fa[set][m], fb[set][n], acc[m][n]);
#pragma unroll
    for (int m = 0; m < 2; ++m) { xs[m] = mfma(fa[set][m], ones, xs[m]); xq[m] = mfma(fa[set][m], fa[set][m], xq[m]); }
  };
  bar();
  STAMP(0, 1);
  consumer_loop<5, 4, 4, 7, 14>(rd, cp);
  STAMP(0, 2);
  float mean[2][4], rs[2][4];
#pragma unroll
  for (int m = 0; m < 2; ++m)
#pragma unroll
    for (int j = 0; j < 4; ++j) {
      const float sq = __shfl(xq[m][j], fq * 20 + j);
      const float mu = xs[m][j] * (1.0f / D);
      const float var = fmaxf(sq * (1.0f / D) - mu * mu, 0.f);
      const float r = __builtin_amdgcn_rsqf(var + 1e-5f);
      mean[m][j] = mu; rs[m][j] = half ? -r : r;
    }
  bar();
  const unsigned dst = WS_ABF + (unsigned)((g * 64 + ts * 4 + w) * 5) * 1024 + lane * 16;
#pragma unroll
  for (int n = 0; n < 5; ++n) {
    const int c = 256 + n * 16 + fr;
    const float wsm = (stat[c] + stat[c + 80]) + (stat[c + 160] + stat[c + 240]);
    float v[2][4];
#pragma unroll
    for (int m = 0; m < 2; ++m)
#pragma unroll
      for (int j = 0; j < 4; ++j) v[m][j] = rs[m][j] * (acc[m][n][j] - mean[m][j] * wsm);
    u32x4 o; o.x = pk2(v[0][0], v[0][1]); o.y = pk2(v[0][2], v[0][3]); o.z = pk2(v[1][0], v[1][1]); o.w = pk2(v[1][2], v[1][3]);
    st16(wr, dst + n * 1024, o);
  }
  STAMP(0, 3); PWAIT(); STAMP(0, 4);
}

__global__ __launch_bounds__(512, 2) void k2_kernel(const bf16_t* __restrict__ x, const bf16_t* __restrict__ Cre, const bf16_t* __restrict__ Cim, char* __restrict__ ws) {
  extern __shared__ __attribute__((aligned(16))) char smem_[];
  LAS char* smem = (LAS char*)smem_;
  STAMP(1, 0);
  const int tid = threadIdx.x, lane = tid & 63, w8 = __builtin_amdgcn_readfirstlane(tid >> 6), w = w8 & 3;
  const int bid = blockIdx.x, xcd = bid & 7, idx = bid >> 3;
  const int half = xcd >> 2, te = idx & 7, tt = (xcd & 3) * 4 + (idx >> 3), g = half * 8 + te;
  const int fr = lane & 15, fq = lane >> 4;
  if (w8 >= 4) {
    const int r8 = lane >> 3, c8 = lane & 7;
    const bf16_t* ca = (half ? Cim : Cre) + (size_t)(tt * 128 + w * 32 + r8) * S;
    const bf16_t* Abase = ca + ((c8 ^ (r8 >> 1)) << 3);
    const bf16_t* AbaseO = ca + 8 * S + ((c8 ^ (4 + (r8 >> 1))) << 3);
    const bf16_t* Bbase = (const bf16_t*)(ws + WS_ABF) + (size_t)g * 64 * 5 * 512 + w * 5 * 512 + lane * 8;
    auto issue = [&](int s, int slot) {
      LAS char* d = smem + slot * ST128;
      s = (s + ROT * te) & 15;
#pragma unroll
      for (int p = 0; p < 5; ++p) glds16(Bbase + (size_t)s * 10240 + p * 512, d + 32768 + (w * 5 + p) * 1024);
#pragma unroll
      for (int sub = 0; sub < 2; ++sub)
#pragma unroll
        for (int p = 0; p < 4; ++p) glds16(((p & 1) ? AbaseO : Abase) + (p >> 1) * 16 * S + s * 128 + sub * 64, d + sub * 16384 + (w * 4 + p) * 1024);
    };
    loader_loop<16, 13>(issue, [](int, int) {});
    return;
  }
  const int rd0 = fr * 128 + ((fq ^ (fr >> 1)) << 4);
  const int rd1 = fr * 128 + (((fq + 4) ^ (fr >> 1)) << 4);
  f32x4 acc[2][5];
#pragma unroll
  for (int m = 0; m < 2; ++m)
#pragma unroll
    for (int n = 0; n < 5; ++n) acc[m][n] = (f32x4){0, 0, 0, 0};
  const int keep = half, send = 1 - half;
  const int trow = tt * 128 + w * 32 + keep * 16 + fr;
  u32x2 xv[5];
#pragma unroll
  for (int n = 0; n < 5; ++n) xv[n] = *(const u32x2*)(x + (size_t)trow * D + te * 80 + n * 16 + fq * 4);
  bf16x8 fa[2][2], fb[2][5];
  auto rd = [&](int slot, int ks, int set) {
    LAS char* d = smem + slot * ST128;
    const int r = (ks & 1) ? rd1 : rd0;
#pragma unroll
    for (int m = 0; m < 2; ++m) fa[set][m] = *(LAS bf16x8*)(d + (ks >> 1) * 16384 + (w * 2 + m) * 2048 + r);
#pragma unroll
    for (int n = 0; n < 5; ++n) fb[set][n] = *(LAS bf16x8*)(d + 32768 + (ks * 5 + n) * 1024 + lane * 16);
  };
  auto cp = [&](int set) {
#pragma unroll
    for (int n = 0; n < 5; ++n)
#pragma unroll
      for (int m = 0; m < 2; ++m) acc[m][n] = mfma(fb[set][n], fa[set][m], acc[m][n]);
  };
  bar();
  STAMP(1, 1);
  consumer_loop<16, 4, 4, 7, 10>(rd, cp);
  STAMP(1, 2);
  const int tile = tt * 8 + te;
  __amdgpu_buffer_rsrc_t wr = mkrsrc(ws, WS_END);
  {
    const unsigned off = WS_XB + (unsigned)(((tile * 2 + send) * 4 + w) * 5) * 1024 + lane * 16;
#pragma unroll
    for (int n = 0; n < 5; ++n) {
      f32x4 v = half ? acc[0][n] : acc[1][n];
      __builtin_amdgcn_raw_buffer_store_b128(__builtin_bit_cast(u32x4, v), wr, off + n * 1024, 0, 16);
    }
  }
  f32x4 kp[5];
#pragma unroll
  for (int n = 0; n < 5; ++n) {
    kp[n] = half ? acc[1][n] : acc[0][n];
    kp[n][0] += bflo(xv[n].x); kp[n][1] += bfhi(xv[n].x); kp[n][2] += bflo(xv[n].y); kp[n][3] += bfhi(xv[n].y);
  }
  wait_v<0>();
  STAMP(1, 3);
  GAS unsigned* flags = (GAS unsigned*)(ws + WS_FL);
  if (lane == 0) __hip_atomic_store(flags + ((tile * 2 + send) * 4 + w) * 16, 1u, __ATOMIC_RELAXED, __HIP_MEMORY_SCOPE_AGENT);
  {
    GAS unsigned* f = flags + ((tile * 2 + keep) * 4 + w) * 16;
    int spins = 0;
    while (__builtin_amdgcn_readfirstlane(__hip_atomic_load(f, __ATOMIC_RELAXED, __HIP_MEMORY_SCOPE_AGENT)) != 1u && ++spins < (1 << 22)) __builtin_amdgcn_s_sleep(1);
    __builtin_amdgcn_fence(__ATOMIC_ACQUIRE, "wavefront");
  }
  STAMP(1, 4);
  float hv[5][4]; float sum = 0.f;
  unsigned hp[5][2];
  {
    const unsigned off = WS_XB + (unsigned)(((tile * 2 + keep) * 4 + w) * 5) * 1024 + lane * 16;
    u32x4 pv[5];
#pragma unroll
    for (int n = 0; n < 5; ++n) pv[n] = __builtin_amdgcn_raw_buffer_load_b128(wr, off + n * 1024, 0, 16);
    const int mt = w * 2 + keep;
#pragma unroll
    for (int n = 0; n < 5; ++n) {
      f32x4 v = kp[n] + __builtin_bit_cast(f32x4, pv[n]);
      hp[n][0] = pk2(v[0], v[1]); hp[n][1] = pk2(v[2], v[3]);
      hv[n][0] = bflo(hp[n][0]); hv[n][1] = bfhi(hp[n][0]); hv[n][2] = bflo(hp[n][1]); hv[n][3] = bfhi(hp[n][1]);
      sum += (hv[n][0] + hv[n][1]) + (hv[n][2] + hv[n][3]);
    }
    const unsigned hb = WS_HF + (unsigned)((tt * 20 + ((te * 5) >> 1)) * 8 + mt) * 1024 + lane * 16;
    if (te & 1) {
      const u32x4 o0 = pairfrag(hp[0][0], hp[0][1], hp[0][0], hp[0][1]);
      st16(wr, lane >= 32 ? hb : 0x80000000u, o0);
      st16(wr, hb + 8192, pairfrag(hp[1][0], hp[1][1], hp[2][0], hp[2][1]));
      st16(wr, hb + 16384, pairfrag(hp[3][0], hp[3][1], hp[4][0], hp[4][1]));
    } else {
      st16(wr, hb, pairfrag(hp[0][0], hp[0][1], hp[1][0], hp[1][1]));
      st16(wr, hb + 8192, pairfrag(hp[2][0], hp[2][1], hp[3][0], hp[3][1]));
      const u32x4 o4 = pairfrag(hp[4][0], hp[4][1], hp[4][0], hp[4][1]);
      st16(wr, lane < 32 ? hb + 16384 : 0x80000000u, o4);
    }
  }
  STAMP(1, 5);
  const float mu = qsum(sum) * (1.0f / 80.0f);
  float m2 = 0.f;
#pragma unroll
  for (int n = 0; n < 5; ++n)
#pragma unroll
    for (int j = 0; j < 4; ++j) { float d = hv[n][j] - mu; m2 += d * d; }
  m2 = qsum(m2);
  if (lane < 16) st8(wr, WS_ST + (unsigned)(te * S + trow) * 8, (u32x2){__float_as_uint(mu), __float_as_uint(m2)});
  STAMP(1, 6); PWAIT(); STAMP(1, 7);
}

constexpr int ST3 = 44 * 1024;
__global__ __launch_bounds__(512, 2) void k3_kernel(char* __restrict__ ws) {
  extern __shared__ __attribute__((aligned(16))) char smem_[];
  LAS char* smem = (LAS char*)smem_;
  STAMP(2, 0);
  const int tid = threadIdx.x, lane = tid & 63, w8 = __builtin_amdgcn_readfirstlane(tid >> 6), w = w8 & 3;
  const int bid = blockIdx.x, xcd = bid & 7, idx = bid >> 3;
  const int rb = (xcd & 1) * 8 + (idx >> 2), tf = (xcd >> 1) * 4 + (idx & 3);
  const int tau0 = (tf * 27) >> 2, ntau = (((tf + 1) * 27) >> 2) - tau0;
  const int fr = lane & 15, fq = lane >> 4;
  LAS float* cs_l = (LAS float*)(smem + 3 * ST3);
  if (w8 >= 4) {
    const int kk = w >> 1;
    const char* hsrc = ws + WS_HF + (size_t)(rb * 20 + kk) * 8192 + lane * 16;
    const char* wsrc = ws + WS_WF + (size_t)(kk * 108 + tau0) * 2048 + lane * 16;
    const char* src0 = (w & 1) ? wsrc + 3 * 1024 : hsrc;
    const char* src1 = (w & 1) ? wsrc + 11 * 1024 : wsrc;
    const unsigned st0 = (w & 1) ? 2 * 108 * 2048 : 2 * 8192;
    auto issue = [&](int s, int slot) {
      LAS char* d = smem + slot * ST3 + kk * 22528 + (w & 1) * 11264;
      const char* a = src0 + (size_t)s * st0;
      const char* b = src1 + (size_t)s * (2 * 108 * 2048);
#pragma unroll
      for (int p = 0; p < 8; ++p) glds16(a + p * 1024, d + p * 1024);
#pragma unroll
      for (int p = 0; p < 3; ++p) glds16(b + p * 1024, d + 8192 + p * 1024);
    };
    const int rot3 = ROT3 ? ((idx & 3) * 2 + (idx >> 2)) % 10 : 0;
    auto issue_r = [&](int s, int slot) { s += rot3; if (s >= 10) s -= 10; issue(s, slot); };
    const int t4 = tid & 255;
    const int c = t4 < 224 ? t4 : 0;
    const float* cp = (const float*)(ws + WS_CS) + ((c >> 4) & 1) * F + (tau0 + (c >> 5)) * 16 + (c & 15);
    const f32x2* sp = (const f32x2*)(ws + WS_ST) + rb * 128 + (t4 & 127);
    float cpart[10]; f32x2 spart[8];
    issue_r(0, 0); issue_r(1, 1);
    wait_v<11>(); bar();
    issue_r(2, 2);
    int slot = 0;
#pragma unroll 1
    for (int s = 0; s < 7; ++s) {
      wait_v<11>(); bar();
      issue_r(s + 3, slot);
      slot = slot == 2 ? 0 : slot + 1;
    }
    wait_v<11>(); bar();
#pragma unroll
    for (int kb = 0; kb < 10; ++kb) cpart[kb] = cp[kb * 2 * F];
#pragma unroll
    for (int i = 0; i < 8; ++i) spart[i] = sp[i * S];
    wait_v<18>(); bar();
    {
      float csum = 0.f;
#pragma unroll
      for (int kb = 0; kb < 10; ++kb) csum += cpart[kb];
      cs_l[t4] = csum;
      const f32x4 a = {spart[0][0], spart[0][1], spart[1][0], spart[1][1]}, b = {spart[2][0], spart[2][1], spart[3][0], spart[3][1]};
      const f32x4 c4 = {spart[4][0], spart[4][1], spart[5][0], spart[5][1]}, d = {spart[6][0], spart[6][1], spart[7][0], spart[7][1]};
      float mean = ((a[0] + a[2]) + (b[0] + b[2]) + (c4[0] + c4[2]) + (d[0] + d[2])) * 0.125f;
      float q = 0.f, e;
      e = a[0] - mean; q += e * e; e = a[2] - mean; q += e * e; e = b[0] - mean; q += e * e; e = b[2] - mean; q += e * e;
      e = c4[0] - mean; q += e * e; e = c4[2] - mean; q += e * e; e = d[0] - mean; q += e * e; e = d[2] - mean; q += e * e;
      float m2 = ((a[1] + a[3]) + (b[1] + b[3]) + (c4[1] + c4[3]) + (d[1] + d[3])) + 80.0f * q;
      if (t4 < 128) *(LAS f32x2*)(cs_l + 256 + t4 * 2) = (f32x2){-mean, __builtin_amdgcn_rsqf(m2 * (1.0f / D) + 1e-5f)};
      wait_l0(); bar();
    }
    return;
  }
  f32x4 acc[2][14];
#pragma unroll
  for (int m = 0; m < 2; ++m)
#pragma unroll
    for (int n = 0; n < 14; ++n) acc[m][n] = (f32x4){0, 0, 0, 0};
  bf16x8 fa[2][2], fb[2][7];
  auto rd = [&](int slot, int ks, int set) {
    LAS char* d = smem + slot * ST3 + (ks >> 1) * 22528 + lane * 16;
#pragma unroll
    for (int m = 0; m < 2; ++m) fa[set][m] = *(LAS bf16x8*)(d + (w * 2 + m) * 1024);
#pragma unroll
    for (int n = 0; n < 7; ++n) fb[set][n] = *(LAS bf16x8*)(d + 8192 + ((ks & 1) * 7 + n) * 1024);
  };
  auto cp = [&](int set) {
#pragma unroll
    for (int n = 0; n < 7; ++n)
#pragma unroll
      for (int m = 0; m < 2; ++m) acc[m][set * 7 + n] = mfma(fb[set][n], fa[set][m], acc[m][set * 7 + n]);
  };
  bar();
  STAMP(2, 1);
  consumer_loop<10, 4, 4, 9, 14>(rd, cp);
  STAMP(2, 2);
  __amdgpu_buffer_rsrc_t wr = mkrsrc(ws, WS_END);
  const int T0 = rb * 8 + w * 2;
  const unsigned obase = WS_HHF + (unsigned)(((T0 >> 2) * 54 + (tau0 >> 1)) * 4 + (T0 & 3)) * 1024 + lane * 16;
  bar();
  float nmu[2], rr[2], rg[2];
#pragma unroll
  for (int m = 0; m < 2; ++m) { const f32x2 v = *(LAS f32x2*)(cs_l + 256 + (w * 32 + m * 16 + fr) * 2); nmu[m] = v[0]; rr[m] = v[1]; rg[m] = v[1] * -1.44269504f; }
  unsigned op[2][7][2];
#pragma unroll
  for (int t = 0; t < 7; ++t) {
    const f32x4 cg = *(LAS f32x4*)(cs_l + t * 32 + fq * 4), cu = *(LAS f32x4*)(cs_l + t * 32 + 16 + fq * 4);
#pragma unroll
    for (int m = 0; m < 2; ++m) {
      const f32x4 gg = (acc[m][2 * t] + cg * nmu[m]) * rg[m];
      const f32x4 uu = (acc[m][2 * t + 1] + cu * nmu[m]) * rr[m];
      f32x4 sg;
#pragma unroll
      for (int j = 0; j < 4; ++j) sg[j] = __builtin_amdgcn_rcpf(1.0f + __builtin_amdgcn_exp2f(gg[j]));
      const f32x4 o = uu * sg;
      op[m][t][0] = pk2(o[0], o[1]); op[m][t][1] = pk2(o[2], o[3]);
    }
  }
  if (tau0 & 1) {
#pragma unroll
    for (int m = 0; m < 2; ++m) {
      st16(wr, lane >= 32 ? obase + m * 1024 : 0x80000000u, pairfrag(op[m][0][0], op[m][0][1], op[m][0][0], op[m][0][1]));
#pragma unroll
      for (int p = 0; p < 3; ++p) st16(wr, obase + m * 1024 + (p + 1) * 4096, pairfrag(op[m][2 * p + 1][0], op[m][2 * p + 1][1], op[m][2 * p + 2][0], op[m][2 * p + 2][1]));
    }
  } else {
#pragma unroll
    for (int m = 0; m < 2; ++m) {
#pragma unroll
      for (int p = 0; p < 3; ++p) st16(wr, obase + m * 1024 + p * 4096, pairfrag(op[m][2 * p][0], op[m][2 * p][1], op[m][2 * p + 1][0], op[m][2 * p + 1][1]));
      st16(wr, lane < 32 ? obase + m * 1024 + 3 * 4096 : 0x80000000u, pairfrag(op[m][6][0], op[m][6][1], op[m][6][0], op[m][6][1]));
    }
  }
  STAMP(2, 3); PWAIT(); STAMP(2, 4);
}

constexpr int ST4 = 36 * 1024;
__global__ __launch_bounds__(512, 2) void k4_kernel(const char* __restrict__ ws, bf16_t* __restrict__ out) {
  extern __shared__ __attribute__((aligned(16))) char smem_[];
  LAS char* smem = (LAS char*)smem_;
  STAMP(3, 0);
  const int tid = threadIdx.x, lane = tid & 63, w8 = __builtin_amdgcn_readfirstlane(tid >> 6), w = w8 & 3;
  const int bid = blockIdx.x, xcd = bid & 7, idx = bid >> 3;
  const int tb = xcd * 4 + (idx >> 3), te = idx & 7;
  const int fr = lane & 15, fq = lane >> 4;
  if (w8 >= 4) {
    const char* asrc = ws + WS_HHF + (size_t)tb * 54 * 4096 + lane * 16;
    const char* bsrc = ws + WS_WDF + (size_t)te * 54 * 5120 + lane * 16;
    auto issue = [&](int s, int slot) {
      int k4 = 4 * s + w; if (k4 > 53) k4 = 53;
      LAS char* d = smem + slot * ST4 + w * 9216;
#pragma unroll
      for (int p = 0; p < 4; ++p) glds16(asrc + (size_t)k4 * 4096 + p * 1024, d + p * 1024);
#pragma unroll
      for (int p = 0; p < 5; ++p) glds16(bsrc + (size_t)k4 * 5120 + p * 1024, d + 4096 + p * 1024);
    };
    loader_loop<14, 9>(issue, [](int, int) {});
    bar();
    return;
  }
  const int mh = w >> 1, kh = w & 1;
  const int T = tb * 4 + w;
  u32x2 hres[5];
#pragma unroll
  for (int n = 0; n < 5; ++n) { const int tau = te * 5 + n; hres[n] = *(const u32x2*)(ws + WS_HF + (size_t)(((T >> 3) * 20 + (tau >> 1)) * 8 + (T & 7)) * 1024 + (((tau & 1) * 2 + (fq >> 1)) * 16 + fr) * 16 + (fq & 1) * 8); }
  f32x4 acc[2][5];
#pragma unroll
  for (int m = 0; m < 2; ++m)
#pragma unroll
    for (int n = 0; n < 5; ++n) acc[m][n] = (f32x4){0, 0, 0, 0};
  bf16x8 fa[2][2], fb[2][5];
  auto rd = [&](int slot, int ks, int set) {
    LAS char* d = smem + slot * ST4 + (ks * 2 + kh) * 9216 + lane * 16;
#pragma unroll
    for (int m = 0; m < 2; ++m) fa[set][m] = *(LAS bf16x8*)(d + (mh * 2 + m) * 1024);
#pragma unroll
    for (int n = 0; n < 5; ++n) fb[set][n] = *(LAS bf16x8*)(d + 4096 + n * 1024);
  };
  auto cp = [&](int set) {
#pragma unroll
    for (int n = 0; n < 5; ++n)
#pragma unroll
      for (int m = 0; m < 2; ++m) acc[m][n] = mfma(fb[set][n], fa[set][m], acc[m][n]);
  };
  bar();
  STAMP(3, 1);
  consumer_loop<14, 2, 1, 7, 10>(rd, cp);
  STAMP(3, 2);
  LAS f32x4* red = (LAS f32x4*)(smem + 2 * ST4);
#pragma unroll
  for (int n = 0; n < 5; ++n) red[((w ^ 1) * 5 + n) * 64 + lane] = kh ? acc[0][n] : acc[1][n];
  wait_l0(); bar();
  unsigned op[5][2];
#pragma unroll
  for (int n = 0; n < 5; ++n) {
    f32x4 v = (kh ? acc[1][n] : acc[0][n]) + red[(w * 5 + n) * 64 + lane];
    v[0] += bflo(hres[n].x); v[1] += bfhi(hres[n].x); v[2] += bflo(hres[n].y); v[3] += bfhi(hres[n].y);
    op[n][0] = pk2(v[0], v[1]); op[n][1] = pk2(v[2], v[3]);
  }
  bf16_t* orow = out + (size_t)(T * 16 + fr) * D + te * 80;
  *(u32x4*)(orow + fq * 8) = pairfrag(op[0][0], op[0][1], op[1][0], op[1][1]);
  *(u32x4*)(orow + 32 + fq * 8) = pairfrag(op[2][0], op[2][1], op[3][0], op[3][1]);
  *(u32x2*)(orow + 64 + fq * 4) = (u32x2){op[4][0], op[4][1]};
  STAMP(3, 3); PWAIT(); STAMP(3, 4);
}

extern "C" void kernel_launch(void* const* d_in, const int* in_sizes, int n_in, void* d_out, int out_size, void* d_ws, size_t ws_size, hipStream_t stream) {
  const bf16_t* x = (const bf16_t*)d_in[0];
  const bf16_t* Cre = (const bf16_t*)d_in[1]; const bf16_t* Cim = (const bf16_t*)d_in[2];
  const bf16_t* Wre = (const bf16_t*)d_in[3]; const bf16_t* Wim = (const bf16_t*)d_in[4];
  const bf16_t* Wg = (const bf16_t*)d_in[6]; const bf16_t* Wu = (const bf16_t*)d_in[7]; const bf16_t* Wd = (const bf16_t*)d_in[8];
  char* ws = (char*)d_ws;
  (void)hipFuncSetAttribute((const void*)k1_kernel, hipFuncAttributeMaxDynamicSharedMemorySize, 3 * ST128 + 4096);
  (void)hipFuncSetAttribute((const void*)k2_kernel, hipFuncAttributeMaxDynamicSharedMemorySize, 3 * ST128);
  (void)hipFuncSetAttribute((const void*)k3_kernel, hipFuncAttributeMaxDynamicSharedMemorySize, 3 * ST3 + 2048);
  (void)hipFuncSetAttribute((const void*)k4_kernel, hipFuncAttributeMaxDynamicSharedMemorySize, 3 * ST4);
  k1_kernel<<<256, 512, 3 * ST128 + 4096, stream>>>(x, Wre, Wim, Cre, Cim, Wg, Wu, Wd, ws);
  k2_kernel<<<256, 512, 3 * ST128, stream>>>(x, Cre, Cim, ws);
  k3_kernel<<<256, 512, 3 * ST3 + 2048, stream>>>(ws);
  k4_kernel<<<256, 512, 3 * ST4, stream>>>(ws, (bf16_t*)d_out);
}
```

```cpp
#include <hip/hip_runtime.h>
#include <hip/hip_bf16.h>
typedef unsigned short bf16_t;
typedef unsigned long long u64;
typedef short bf16x8 __attribute__((ext_vector_type(8)));
typedef float f32x4 __attribute__((ext_vector_type(4)));
typedef float f32x2 __attribute__((ext_vector_type(2)));
typedef unsigned u32x4 __attribute__((ext_vector_type(4)));
typedef unsigned u32x2 __attribute__((ext_vector_type(2)));
typedef __bf16 bf16x2_t __attribute__((ext_vector_type(2)));
#define LAS __attribute__((address_space(3)))
#define GAS __attribute__((address_space(1)))
#define DEV __device__ __forceinline__

constexpr int S = 2048, D = 640, F = 1728;
constexpr unsigned WS_ABF = 0;
constexpr unsigned WS_HF = WS_ABF + 5242880;
constexpr unsigned WS_WF = WS_HF + 2621440;
constexpr unsigned WS_HHF = WS_WF + 4423680;
constexpr unsigned WS_WDF = WS_HHF + 7077888;
constexpr unsigned WS_XB = WS_WDF + 2211840;
constexpr unsigned WS_CS = WS_XB + 5242880;
constexpr unsigned WS_ST = WS_CS + 138240;
constexpr unsigned WS_FL = WS_ST + 131072;
constexpr unsigned FL_E = WS_FL, FL_F1 = WS_FL + 256, FL_F2 = FL_F1 + 65536, FL_F3 = FL_F2 + 65536, FL_FW = FL_F3 + 65536, FL_FX = FL_FW + 8192;
constexpr unsigned WS_END = FL_FX + 131072;
constexpr int ST = 52 * 1024;
constexpr int LDS_BYTES = 3 * ST + 4096;
#define ROT 2

template <int AUX> DEV void glds16(const void* g, LAS void* l) { __builtin_amdgcn_global_load_lds((const GAS unsigned*)g, (LAS unsigned*)l, 16, 0, AUX); }
template <int N> DEV void wait_v() { asm volatile("s_waitcnt vmcnt(%0)" ::"i"(N) : "memory"); }
DEV void wait_l0() { asm volatile("s_waitcnt lgkmcnt(0)" ::: "memory"); }
DEV void bar() { asm volatile("" ::: "memory"); __builtin_amdgcn_s_barrier(); asm volatile("" ::: "memory"); }
DEV f32x4 mfma(bf16x8 a, bf16x8 b, f32x4 c) { return __builtin_amdgcn_mfma_f32_16x16x32_bf16(a, b, c, 0, 0, 0); }
DEV unsigned pk2(float lo, float hi) { bf16x2_t v; v.x = (__bf16)lo; v.y = (__bf16)hi; return __builtin_bit_cast(unsigned, v); }
DEV float bflo(unsigned u) { return __uint_as_float(u << 16); }
DEV float bfhi(unsigned u) { return __uint_as_float(u & 0xffff0000u); }
DEV float dot2(unsigned a, unsigned b, float c) { return __builtin_amdgcn_fdot2_f32_bf16(__builtin_bit_cast(bf16x2_t, a), __builtin_bit_cast(bf16x2_t, b), c, false); }
typedef __amdgpu_buffer_rsrc_t rsrc_t;
DEV rsrc_t mkrsrc(const void* p, unsigned bytes) { return __builtin_amdgcn_make_buffer_rsrc((void*)p, 0, bytes, 0x00020000); }
DEV void st16(rsrc_t r, unsigned off, u32x4 v) { __builtin_amdgcn_raw_buffer_store_b128(v, r, off, 0, 16); }
DEV void st8(rsrc_t r, unsigned off, u32x2 v) { __builtin_amdgcn_raw_buffer_store_b64(v, r, off, 0, 16); }
DEV void st4(rsrc_t r, unsigned off, unsigned v) { __builtin_amdgcn_raw_buffer_store_b32(v, r, off, 0, 16); }
DEV u32x4 ld16(rsrc_t r, unsigned off) { return __builtin_amdgcn_raw_buffer_load_b128(r, off, 0, 16); }
DEV u32x2 ld8(rsrc_t r, unsigned off) { return __builtin_amdgcn_raw_buffer_load_b64(r, off, 0, 16); }
DEV unsigned ld4(rsrc_t r, unsigned off) { return __builtin_amdgcn_raw_buffer_load_b32(r, off, 0, 16); }
DEV void publish(char* ws, unsigned off, u64 ep, int lane) {
  if (lane == 0) __hip_atomic_store((GAS u64*)(ws + off), ep, __ATOMIC_RELAXED, __HIP_MEMORY_SCOPE_AGENT);
}
DEV void publish_wg(char* ws, unsigned off, u64 ep, int lane, LAS unsigned* cnt) {
  unsigned old = 0;
  if (lane == 0) old = __hip_atomic_fetch_add(cnt, 1u, __ATOMIC_RELAXED, __HIP_MEMORY_SCOPE_WORKGROUP);
  if (__builtin_amdgcn_readfirstlane(old) == 3u) publish(ws, off, ep, lane);
}
DEV void await(char* ws, unsigned off  , u64 ep) {
  GAS u64* p = (GAS u64*)(ws + off);
#pragma unroll 1
  for (int spins = 0; spins < (1 << 16); ++spins) {
    const u64 v = __hip_atomic_load(p, __ATOMIC_RELAXED, __HIP_MEMORY_SCOPE_AGENT);
    if (__all(v == ep)) break;
    __builtin_amdgcn_s_sleep(2);
  }
  __builtin_amdgcn_fence(__ATOMIC_ACQUIRE, "wavefront");
  asm volatile("" ::: "memory");
}
DEV float qsum(float v) {
  auto a = __builtin_amdgcn_permlane16_swap(__float_as_uint(v), __float_as_uint(v), false, false);
  v = __uint_as_float(a[0]) + __uint_as_float(a[1]);
  auto b = __builtin_amdgcn_permlane32_swap(__float_as_uint(v), __float_as_uint(v), false, false);
  return __uint_as_float(b[0]) + __uint_as_float(b[1]);
}
DEV u32x4 pairfrag(unsigned a0, unsigned a1, unsigned b0, unsigned b1) {
  auto p0 = __builtin_amdgcn_permlane32_swap(a0, b0, false, false);
  auto p1 = __builtin_amdgcn_permlane32_swap(a1, b1, false, false);
  auto q0 = __builtin_amdgcn_permlane16_swap(p0[0], p0[1], false, false);
  auto q1 = __builtin_amdgcn_permlane16_swap(p1[0], p1[1], false, false);
  return (u32x4){q0[0], q1[0], q0[1], q1[1]};
}
#define STAMP(k, i)
#define LST(i)
#define SB0() __builtin_amdgcn_sched_barrier(0)
DEV int nxt(int slot) { return slot == 2 ? 0 : slot + 1; }
template <int NRD, int NMF> DEV void interleave() {
#pragma unroll
  for (int i = 0; i < NRD; ++i) { __builtin_amdgcn_sched_group_barrier(0x8, 1, 0); __builtin_amdgcn_sched_group_barrier(0x100, 1, 0); }
  __builtin_amdgcn_sched_group_barrier(0x8, NMF - NRD, 0);
}
template <int NS, int KS, int KSL, int NRD, int NMF, bool ENDBAR, class Rd, class Cp, class Mid>
DEV void consumer_loop(int slot, Rd rd, Cp cp, Mid mid) {
  rd(slot, 0, 0);
  SB0();
#pragma unroll 1
  for (int s = 0; s < NS - 1; ++s) {
    const int nslot = nxt(slot);
    mid(s);
#pragma unroll
    for (int ks = 0; ks < KS - 1; ++ks) { rd(slot, ks + 1, (ks + 1) & 1); cp(ks & 1); interleave<NRD, NMF>(); SB0(); }
    wait_l0(); bar();
    rd(nslot, 0, 0); cp(1); interleave<NRD, NMF>(); SB0();
    slot = nslot;
  }
#pragma unroll
  for (int ks = 0; ks < KSL - 1; ++ks) { rd(slot, ks + 1, (ks + 1) & 1); cp(ks & 1); interleave<NRD, NMF>(); SB0(); }
  if (ENDBAR) { wait_l0(); bar(); }
  cp((KSL - 1) & 1);
  SB0();
}

__global__ __launch_bounds__(512, 2) void fnet_glu_kernel(const bf16_t* __restrict__ x, const bf16_t* __restrict__ Cre, const bf16_t* __restrict__ Cim,
                                                          const bf16_t* __restrict__ Wre, const bf16_t* __restrict__ Wim, const bf16_t* __restrict__ Wg,
                                                          const bf16_t* __restrict__ Wu, const bf16_t* __restrict__ Wd, char* ws, bf16_t* __restrict__ out) {
  extern __shared__ __attribute__((aligned(16))) char smem_[];
  LAS char* smem = (LAS char*)smem_;
  STAMP(0, 0);
  const int tid = threadIdx.x, lane = tid & 63, w8 = __builtin_amdgcn_readfirstlane(tid >> 6), w = w8 & 3;
  const int bid = blockIdx.x, xcd = bid & 7, idx = bid >> 3;
  const int half = xcd >> 2, te = idx & 7, tt = (xcd & 3) * 4 + (idx >> 3), g = half * 8 + te;
  const int rb = (xcd & 1) * 8 + (idx >> 2), tf = (xcd >> 1) * 4 + (idx & 3), tau0 = (tf * 27) >> 2;
  const int tb = xcd * 4 + (idx >> 3);
  const int fr = lane & 15, fq = lane >> 4, r8 = lane >> 3, c8 = lane & 7;
  const int rd0 = fr * 128 + ((fq ^ (fr >> 1)) << 4), rd1 = fr * 128 + (((fq + 4) ^ (fr >> 1)) << 4);
  LAS float* misc = (LAS float*)(smem + 3 * ST);
  const rsrc_t wr = mkrsrc(ws, WS_END);
  u64 ep;
  if (w8 >= 4) {
    const int sE = (c8 ^ (r8 >> 1)) << 3, sO = (c8 ^ (4 + (r8 >> 1))) << 3;
    const int rowE = 8 * (r8 >> 2) + (r8 & 3);
    const bf16_t* xa = x + (size_t)(tt * 128 + w * 32) * D;
    const bf16_t* A1 = xa + rowE * D + sE;
    const bf16_t* A1o = xa + (rowE + 16) * D + sO;
    const bf16_t* wb = (half ? Wim : Wre) + (size_t)(te * 80 + (w & 1) * 40 + r8) * D + (w >> 1) * 64;
    const bf16_t* B1 = wb + ((w & 1) ? sO : sE);
    const bf16_t* B1o = wb + ((w & 1) ? sE : sO);
    auto issue1 = [&](int s, int slot) {
      LAS char* d = smem + slot * ST;
#pragma unroll
      for (int sub = 0; sub < 2; ++sub)
#pragma unroll
        for (int p = 0; p < 4; ++p) glds16<0>(((p & 1) ? A1o : A1) + (p >> 1) * 4 * D + s * 128 + sub * 64, d + sub * 16384 + (w * 4 + p) * 1024);
#pragma unroll
      for (int p = 0; p < 5; ++p) glds16<0>(((p & 1) ? B1o : B1) + p * 8 * D + s * 128, d + 32768 + (w * 5 + p) * 1024);
    };
    float wsum[5] = {0.f, 0.f, 0.f, 0.f, 0.f};
    auto post1 = [&](int slot) {
      LAS char* d = smem + slot * ST;
#pragma unroll
      for (int n = 0; n < 5; ++n) {
        u32x4 u = *(LAS u32x4*)(d + 32768 + (w >> 1) * 10240 + n * 2048 + ((w & 1) ? rd1 : rd0));
#pragma unroll
        for (int i = 0; i < 4; ++i) wsum[n] = dot2(u[i], 0x3F803F80u, wsum[n]);
      }
      asm volatile("s_waitcnt lgkmcnt(0)" : "+v"(wsum[0]), "+v"(wsum[1]), "+v"(wsum[2]), "+v"(wsum[3]), "+v"(wsum[4]) :: "memory");
    };
    const bf16_t* ca = (half ? Cim : Cre) + (size_t)(tt * 128 + w * 32 + r8) * S;
    const bf16_t* A2 = ca + sE;
    const bf16_t* A2o = ca + 8 * S + sO;
    const char* B2 = ws + WS_ABF + (size_t)(g * 64 + w) * 5120 + lane * 16;
    auto i2 = [&](int s, int slot) {
      LAS char* d = smem + slot * ST; s = (s + ROT * te) & 15;
#pragma unroll
      for (int sub = 0; sub < 2; ++sub)
#pragma unroll
        for (int p = 0; p < 4; ++p) glds16<0>(((p & 1) ? A2o : A2) + (p >> 1) * 16 * S + s * 128 + sub * 64, d + sub * 16384 + (w * 4 + p) * 1024);
    };
    auto d2 = [&](int s, int slot) {
      LAS char* d = smem + slot * ST; s = (s + ROT * te) & 15;
#pragma unroll
      for (int p = 0; p < 5; ++p) glds16<16>(B2 + (size_t)s * 20480 + p * 1024, d + 32768 + (w * 5 + p) * 1024);
    };
    const char* W3 = ws + WS_WF + (size_t)(((w >> 1) * 108 + tau0) * 2 + (w & 1) * 7) * 1024 + lane * 16;
    const char* H3 = ws + WS_HF + (size_t)((rb * 20 + (w >> 1)) * 8 + (w & 1) * 4) * 1024 + lane * 16;
    auto i3 = [&](int s, int slot) {
      LAS char* d = smem + slot * ST + (w >> 1) * 22528 + 8192 + (w & 1) * 7168;
#pragma unroll
      for (int p = 0; p < 7; ++p) glds16<16>(W3 + (size_t)s * (2 * 108 * 2048) + p * 1024, d + p * 1024);
    };
    auto d3 = [&](int s, int slot) {
      LAS char* d = smem + slot * ST + (w >> 1) * 22528 + (w & 1) * 4096;
#pragma unroll
      for (int p = 0; p < 4; ++p) glds16<16>(H3 + (size_t)s * 16384 + p * 1024, d + p * 1024);
    };
    const char* W4 = ws + WS_WDF + (size_t)te * 54 * 5120 + lane * 16;
    const char* H4 = ws + WS_HHF + (size_t)tb * 54 * 4096 + lane * 16;
    auto i4 = [&](int s, int slot) {
      int k4 = 4 * s + w; if (k4 > 53) k4 = 53;
      LAS char* d = smem + slot * ST + w * 9216 + 4096;
#pragma unroll
      for (int p = 0; p < 5; ++p) glds16<16>(W4 + (size_t)k4 * 5120 + p * 1024, d + p * 1024);
    };
    auto d4 = [&](int s, int slot) {
      int k4 = 4 * s + w; if (k4 > 53) k4 = 53;
      LAS char* d = smem + slot * ST + w * 9216;
#pragma unroll
      for (int p = 0; p < 4; ++p) glds16<16>(H4 + (size_t)k4 * 4096 + p * 1024, d + p * 1024);
    };

    issue1(0, 0); issue1(1, 1);
    wait_v<13>(); bar();
    issue1(2, 2); post1(0);
    wait_v<13>(); bar();
    issue1(3, 0); post1(1);
    { const u32x2 e = *(LAS u32x2*)(misc + 1000); ep = ((u64)__builtin_amdgcn_readfirstlane(e.y) << 32) | (unsigned)__builtin_amdgcn_readfirstlane(e.x); }
    wait_v<13>(); bar();
    issue1(4, 1); post1(2);
    wait_v<13>(); bar();
    i2(0, 2); post1(0);
    wait_v<8>(); bar();
    i2(1, 0); post1(1);
#pragma unroll
    for (int n = 0; n < 5; ++n) { const float v = qsum(wsum[n]); if (lane < 16) misc[w * 80 + n * 16 + lane] = v; }
    wait_l0(); bar();
    i2(2, 1);
    LST(0);
    if (w == 0) await(ws, FL_F1 + (g * 16 + fr) * 256, ep);
    LST(1);
    bar();
    d2(0, 2); d2(1, 0); d2(2, 1);
    wait_v<10>(); bar();
    wait_v<5>(); bar();
    {
      int slot = 2;
#pragma unroll 1
      for (int s = 3; s < 16; ++s) { d2(s, slot); i2(s, slot); slot = nxt(slot); wait_v<13>(); bar(); }
    }
    i3(0, 0);
    wait_v<7>(); bar();
    i3(1, 1);
    bar();
    i3(2, 2);
    LST(2);
    if (w == 0) await(ws, FL_F2 + (rb * 16 + fr) * 256, ep);
    LST(3);
    bar();
    d3(0, 0); d3(1, 1); d3(2, 2);
    wait_v<8>(); bar();
    wait_v<4>(); bar();
    {
      int slot = 0;
#pragma unroll 1
      for (int s = 3; s < 10; ++s) { d3(s, slot); i3(s, slot); slot = nxt(slot); wait_v<11>(); bar(); }
    }
    {
      const int t4 = tid & 255;
      const int c = t4 < 224 ? t4 : 0;
      const unsigned co = WS_CS + (unsigned)(((c >> 4) & 1) * F + (tau0 + (c >> 5)) * 16 + (c & 15)) * 4;
      const unsigned so = WS_ST + (unsigned)(rb * 128 + (t4 & 127)) * 8;
      unsigned cpart[10]; u32x2 sp[8];
#pragma unroll
      for (int kb = 0; kb < 10; ++kb) cpart[kb] = ld4(wr, co + kb * 2 * F * 4);
#pragma unroll
      for (int i = 0; i < 8; ++i) sp[i] = ld8(wr, so + i * S * 8);
      wait_v<18>(); bar();
      float csum = 0.f;
#pragma unroll
      for (int kb = 0; kb < 10; ++kb) csum += __uint_as_float(cpart[kb]);
      misc[t4] = csum;
      float mean = 0.f, m2 = 0.f;
#pragma unroll
      for (int i = 0; i < 8; ++i) { mean += __uint_as_float(sp[i].x); m2 += __uint_as_float(sp[i].y); }
      mean *= 0.125f;
      float q = 0.f;
#pragma unroll
      for (int i = 0; i < 8; ++i) { const float e = __uint_as_float(sp[i].x) - mean; q += e * e; }
      m2 += 80.0f * q;
      if (t4 < 128) *(LAS f32x2*)(misc + 256 + t4 * 2) = (f32x2){-mean, __builtin_amdgcn_rsqf(m2 * (1.0f / D) + 1e-5f)};
      wait_l0();
    }
    i4(0, 1); i4(1, 2);
    bar();
    i4(2, 0);
    LST(4);
    if (w == 0) await(ws, FL_F3 + ((tb >> 1) * 16 + fr) * 256, ep);
    LST(5);
    bar();
    d4(0, 1); d4(1, 2); d4(2, 0);
    wait_v<8>(); bar();
    wait_v<4>(); bar();
    {
      int slot = 1;
#pragma unroll 1
      for (int s = 3; s < 14; ++s) { d4(s, slot); i4(s, slot); slot = nxt(slot); wait_v<9>(); bar(); }
    }
    wait_v<0>(); bar();
    return;
  }

  bf16x8 fa[2][2], fb[2][7];
  {
    const int blk = bid * 4 + w;
    const int mat = blk / 270, rem = blk % 270;
    const bool tvalid = blk < 810;
    int kb, nb, ldw; const bf16_t* wsrc;
    if (mat < 2) { kb = rem / 27; nb = rem % 27; ldw = F; wsrc = mat ? Wu : Wg; } else { kb = rem / 10; nb = rem % 10; ldw = D; wsrc = Wd; }
    if (!tvalid) { kb = 0; nb = 0; }
    u64 e0 = __hip_atomic_load((GAS u64*)(ws + FL_E), __ATOMIC_RELAXED, __HIP_MEMORY_SCOPE_AGENT);
    u32x4 wblk[8];
    {
      const bf16_t* src = wsrc + (size_t)(kb * 64 + r8) * ldw + nb * 64 + c8 * 8;
#pragma unroll
      for (int p = 0; p < 8; ++p) wblk[p] = *(const u32x4*)(src + (size_t)p * 8 * ldw);
    }
    {
      e0 = e0 * 6364136223846793005ull + 1442695040888963407ull;
      const unsigned lo = __builtin_amdgcn_readfirstlane((unsigned)e0), hi = __builtin_amdgcn_readfirstlane((unsigned)(e0 >> 32));
      ep = ((u64)hi << 32) | lo;
      if (tid == 0) { *(LAS u32x2*)(misc + 1000) = (u32x2){lo, hi}; *(LAS u32x4*)(misc + 1004) = (u32x4){0, 0, 0, 0}; }
      wait_l0();
    }
    f32x4 acc[2][5], xs[2], xq[2]; bf16x8 ones;
#pragma unroll
    for (int m = 0; m < 2; ++m) { xs[m] = (f32x4){0, 0, 0, 0}; xq[m] = (f32x4){0, 0, 0, 0};
#pragma unroll
      for (int n = 0; n < 5; ++n) acc[m][n] = (f32x4){0, 0, 0, 0}; }
#pragma unroll
    for (int i = 0; i < 8; ++i) ones[i] = 0x3F80;
    auto rd = [&](int slot, int ks, int set) {
      LAS char* d = smem + slot * ST;
      const int r = (ks & 1) ? rd1 : rd0;
#pragma unroll
      for (int m = 0; m < 2; ++m) fa[set][m] = *(LAS bf16x8*)(d + (ks >> 1) * 16384 + (w * 2 + m) * 2048 + r);
#pragma unroll
      for (int n = 0; n < 5; ++n) fb[set][n] = *(LAS bf16x8*)(d + 32768 + (ks >> 1) * 10240 + n * 2048 + r);
    };
    auto cp = [&](int set) {
#pragma unroll
      for (int n = 0; n < 5; ++n)
#pragma unroll
        for (int m = 0; m < 2; ++m) acc[m][n] = mfma(fa[set][m], fb[set][n], acc[m][n]);
#pragma unroll
      for (int m = 0; m < 2; ++m) { xs[m] = mfma(fa[set][m], ones, xs[m]); xq[m] = mfma(fa[set][m], fa[set][m], xq[m]); }
    };
    bar();
    STAMP(0, 1);
    consumer_loop<5, 4, 4, 7, 14, true>(0, rd, cp, [](int) {});
    STAMP(0, 2);
    float mean[2][4], rs[2][4];
#pragma unroll
    for (int m = 0; m < 2; ++m)
#pragma unroll
      for (int j = 0; j < 4; ++j) {
        const float sq = __shfl(xq[m][j], fq * 20 + j);
        const float mu = xs[m][j] * (1.0f / D);
        const float var = fmaxf(sq * (1.0f / D) - mu * mu, 0.f);
        const float r = __builtin_amdgcn_rsqf(var + 1e-5f);
        mean[m][j] = mu; rs[m][j] = half ? -r : r;
      }
    const unsigned dst = WS_ABF + (unsigned)((g * 64 + tt * 4 + w) * 5) * 1024 + lane * 16;
#pragma unroll
    for (int n = 0; n < 5; ++n) {
      const int c = n * 16 + fr;
      const float wsm = (misc[c] + misc[c + 80]) + (misc[c + 160] + misc[c + 240]);
      float v[2][4];
#pragma unroll
      for (int m = 0; m < 2; ++m)
#pragma unroll
        for (int j = 0; j < 4; ++j) v[m][j] = rs[m][j] * (acc[m][n][j] - mean[m][j] * wsm);
      u32x4 o; o.x = pk2(v[0][0], v[0][1]); o.y = pk2(v[0][2], v[0][3]); o.z = pk2(v[1][0], v[1][1]); o.w = pk2(v[1][2], v[1][3]);
      st16(wr, dst + n * 1024, o);
    }
    STAMP(0, 3);
    LAS char* tbuf = smem + ((w >> 1) ? 0 : 2 * ST) + 32768 + (w & 1) * 8192;
#pragma unroll
    for (int p = 0; p < 8; ++p) *(LAS u32x4*)(tbuf + p * 1024 + lane * 16) = wblk[p];
    wait_l0();
    u32x2 lo[4][2], hi[4][2];
    {
      const unsigned tbase = (unsigned)(size_t)tbuf + ((fq * 8 + (fr >> 2)) * 128 + (fr & 3) * 8);
#pragma unroll
      for (int c = 0; c < 4; ++c)
#pragma unroll
        for (int kk = 0; kk < 2; ++kk)
          asm volatile("ds_read_b64_tr_b16 %0, %2 offset:%3\n\tds_read_b64_tr_b16 %1, %2 offset:%4"
                       : "=&v"(lo[c][kk]), "=&v"(hi[c][kk]) : "v"(tbase), "i"(kk * 4096 + c * 32), "i"(kk * 4096 + 512 + c * 32) : "memory");
      asm volatile("s_waitcnt lgkmcnt(0)" : "+v"(lo[0][0]), "+v"(lo[0][1]), "+v"(lo[1][0]), "+v"(lo[1][1]), "+v"(lo[2][0]), "+v"(lo[2][1]), "+v"(lo[3][0]), "+v"(lo[3][1]),
                   "+v"(hi[0][0]), "+v"(hi[0][1]), "+v"(hi[1][0]), "+v"(hi[1][1]), "+v"(hi[2][0]), "+v"(hi[2][1]), "+v"(hi[3][0]), "+v"(hi[3][1]) :: "memory");
    }
    wait_v<0>();
    publish_wg(ws, FL_F1 + (g * 16 + tt) * 256, ep, lane, (LAS unsigned*)(misc + 1004));
    STAMP(0, 4);
    if (tvalid) {
#pragma unroll
      for (int c = 0; c < 4; ++c) {
        float cs = 0.f;
#pragma unroll
        for (int kk = 0; kk < 2; ++kk) {
          u32x4 o = {lo[c][kk].x, lo[c][kk].y, hi[c][kk].x, hi[c][kk].y};
          unsigned d2;
          if (mat < 2) d2 = WS_WF + (unsigned)(((kb * 2 + kk) * 108 + nb * 4 + c) * 2 + mat) * 1024;
          else { int eps = nb * 4 + c; d2 = WS_WDF + (unsigned)(((eps / 5) * 54 + kb * 2 + kk) * 5 + eps % 5) * 1024; }
          st16(wr, d2 + lane * 16, o);
#pragma unroll
          for (int i = 0; i < 4; ++i) cs = dot2(o[i], 0x3F803F80u, cs);
        }
        cs = qsum(cs);
        if (mat < 2 && lane < 16) st4(wr, WS_CS + (unsigned)((kb * 2 + mat) * F + nb * 64 + c * 16 + lane) * 4, __float_as_uint(cs));
      }
    }
    bar();
    wait_v<0>();
    publish(ws, FL_FW + blk * 8, ep, lane);
    STAMP(0, 5);
  }
  {
    f32x4 acc[2][5];
#pragma unroll
    for (int m = 0; m < 2; ++m)
#pragma unroll
      for (int n = 0; n < 5; ++n) acc[m][n] = (f32x4){0, 0, 0, 0};
    const int keep = half, send = 1 - half;
    const int trow = tt * 128 + w * 32 + keep * 16 + fr;
    u32x2 xv[5];
#pragma unroll
    for (int n = 0; n < 5; ++n) xv[n] = *(const u32x2*)(x + (size_t)trow * D + te * 80 + n * 16 + fq * 4);
    auto rd = [&](int slot, int ks, int set) {
      LAS char* d = smem + slot * ST;
      const int r = (ks & 1) ? rd1 : rd0;
#pragma unroll
      for (int m = 0; m < 2; ++m) fa[set][m] = *(LAS bf16x8*)(d + (ks >> 1) * 16384 + (w * 2 + m) * 2048 + r);
#pragma unroll
      for (int n = 0; n < 5; ++n) fb[set][n] = *(LAS bf16x8*)(d + 32768 + (ks * 5 + n) * 1024 + lane * 16);
    };
    auto cp = [&](int set) {
#pragma unroll
      for (int n = 0; n < 5; ++n)
#pragma unroll
        for (int m = 0; m < 2; ++m) acc[m][n] = mfma(fb[set][n], fa[set][m], acc[m][n]);
    };
    u32x4 fw0 = {0, 0, 0, 0}, fw1 = {0, 0, 0, 0};
    auto mid = [&](int s) {
      if (s == 8) { fw0 = ld16(wr, FL_FW + w * 2048 + lane * 32); fw1 = ld16(wr, FL_FW + w * 2048 + lane * 32 + 16); }
      if (s == 11) {
        const unsigned lo = (unsigned)ep, hi = (unsigned)(ep >> 32);
        const bool ok = fw0.x == lo && fw0.y == hi && fw0.z == lo && fw0.w == hi && fw1.x == lo && fw1.y == hi && fw1.z == lo && fw1.w == hi;
        if (!__all(ok)) {
#pragma unroll 1
          for (int i = 0; i < 4; ++i) await(ws, FL_FW + w * 2048 + i * 512 + lane * 8, ep);
        }
      }
    };
    bar();
    STAMP(1, 1);
    consumer_loop<16, 4, 4, 7, 10, true>(2, rd, cp, mid);
    STAMP(1, 2);
    const int tile = tt * 8 + te;
    {
      const unsigned off = WS_XB + (unsigned)(((tile * 2 + send) * 4 + w) * 5) * 1024 + lane * 16;
#pragma unroll
      for (int n = 0; n < 5; ++n) st16(wr, off + n * 1024, __builtin_bit_cast(u32x4, half ? acc[0][n] : acc[1][n]));
    }
    f32x4 kp[5];
#pragma unroll
    for (int n = 0; n < 5; ++n) {
      kp[n] = half ? acc[1][n] : acc[0][n];
      kp[n][0] += bflo(xv[n].x); kp[n][1] += bfhi(xv[n].x); kp[n][2] += bflo(xv[n].y); kp[n][3] += bfhi(xv[n].y);
    }
    wait_v<0>();
    publish(ws, FL_FX + ((tile * 2 + send) * 4 + w) * 128, ep, lane);
    STAMP(1, 3);
    await(ws, FL_FX + ((tile * 2 + keep) * 4 + w) * 128, ep);
    STAMP(1, 4);
    float hv[5][4]; float sum = 0.f;
    unsigned hp[5][2];
    const unsigned off = WS_XB + (unsigned)(((tile * 2 + keep) * 4 + w) * 5) * 1024 + lane * 16;
    u32x4 pv[5];
#pragma unroll
    for (int n = 0; n < 5; ++n) pv[n] = ld16(wr, off + n * 1024);
#pragma unroll
    for (int n = 0; n < 5; ++n) {
      f32x4 v = kp[n] + __builtin_bit_cast(f32x4, pv[n]);
      hp[n][0] = pk2(v[0], v[1]); hp[n][1] = pk2(v[2], v[3]);
      hv[n][0] = bflo(hp[n][0]); hv[n][1] = bfhi(hp[n][0]); hv[n][2] = bflo(hp[n][1]); hv[n][3] = bfhi(hp[n][1]);
      sum += (hv[n][0] + hv[n][1]) + (hv[n][2] + hv[n][3]);
    }
    const unsigned hb = WS_HF + (unsigned)((tt * 20 + ((te * 5) >> 1)) * 8 + w * 2 + keep) * 1024 + lane * 16;
    if (te & 1) {
      st16(wr, lane >= 32 ? hb : 0x80000000u, pairfrag(hp[0][0], hp[0][1], hp[0][0], hp[0][1]));
      st16(wr, hb + 8192, pairfrag(hp[1][0], hp[1][1], hp[2][0], hp[2][1]));
      st16(wr, hb + 16384, pairfrag(hp[3][0], hp[3][1], hp[4][0], hp[4][1]));
    } else {
      st16(wr, hb, pairfrag(hp[0][0], hp[0][1], hp[1][0], hp[1][1]));
      st16(wr, hb + 8192, pairfrag(hp[2][0], hp[2][1], hp[3][0], hp[3][1]));
      st16(wr, lane < 32 ? hb + 16384 : 0x80000000u, pairfrag(hp[4][0], hp[4][1], hp[4][0], hp[4][1]));
    }
    const float mu = qsum(sum) * (1.0f / 80.0f);
    float m2 = 0.f;
#pragma unroll
    for (int n = 0; n < 5; ++n)
#pragma unroll
      for (int j = 0; j < 4; ++j) { float d = hv[n][j] - mu; m2 += d * d; }
    m2 = qsum(m2);
    if (lane < 16) st8(wr, WS_ST + (unsigned)(te * S + trow) * 8, (u32x2){__float_as_uint(mu), __float_as_uint(m2)});
    STAMP(1, 5);
    wait_v<0>();
    publish_wg(ws, FL_F2 + (tt * 16 + g) * 256, ep, lane, (LAS unsigned*)(misc + 1005));
    STAMP(1, 6);
    bar();
  }
  {
    f32x4 acc[2][14];
#pragma unroll
    for (int m = 0; m < 2; ++m)
#pragma unroll
      for (int n = 0; n < 14; ++n) acc[m][n] = (f32x4){0, 0, 0, 0};
    auto rd = [&](int slot, int ks, int set) {
      LAS char* d = smem + slot * ST + (ks >> 1) * 22528 + lane * 16;
#pragma unroll
      for (int m = 0; m < 2; ++m) fa[set][m] = *(LAS bf16x8*)(d + (w * 2 + m) * 1024);
#pragma unroll
      for (int n = 0; n < 7; ++n) fb[set][n] = *(LAS bf16x8*)(d + 8192 + ((ks & 1) * 7 + n) * 1024);
    };
    auto cp = [&](int set) {
#pragma unroll
      for (int n = 0; n < 7; ++n)
#pragma unroll
        for (int m = 0; m < 2; ++m) acc[m][set * 7 + n] = mfma(fb[set][n], fa[set][m], acc[m][set * 7 + n]);
    };
    bar();
    STAMP(2, 1);
    consumer_loop<10, 4, 4, 9, 14, true>(0, rd, cp, [](int) {});
    STAMP(2, 2);
    const int T0 = rb * 8 + w * 2;
    const unsigned obase = WS_HHF + (unsigned)(((T0 >> 2) * 54 + (tau0 >> 1)) * 4 + (T0 & 3)) * 1024 + lane * 16;
    float nmu[2], rr[2], rg[2];
#pragma unroll
    for (int m = 0; m < 2; ++m) { const f32x2 v = *(LAS f32x2*)(misc + 256 + (w * 32 + m * 16 + fr) * 2); nmu[m] = v[0]; rr[m] = v[1]; rg[m] = v[1] * -1.44269504f; }
    unsigned op[2][7][2];
#pragma unroll
    for (int t = 0; t < 7; ++t) {
      const f32x4 cg = *(LAS f32x4*)(misc + t * 32 + fq * 4), cu = *(LAS f32x4*)(misc + t * 32 + 16 + fq * 4);
#pragma unroll
      for (int m = 0; m < 2; ++m) {
        const f32x4 gg = (acc[m][2 * t] + cg * nmu[m]) * rg[m];
        const f32x4 uu = (acc[m][2 * t + 1] + cu * nmu[m]) * rr[m];
        f32x4 sg;
#pragma unroll
        for (int j = 0; j < 4; ++j) sg[j] = __builtin_amdgcn_rcpf(1.0f + __builtin_amdgcn_exp2f(gg[j]));
        const f32x4 o = uu * sg;
        op[m][t][0] = pk2(o[0], o[1]); op[m][t][1] = pk2(o[2], o[3]);
      }
    }
    if (tau0 & 1) {
#pragma unroll
      for (int m = 0; m < 2; ++m) {
        st16(wr, lane >= 32 ? obase + m * 1024 : 0x80000000u, pairfrag(op[m][0][0], op[m][0][1], op[m][0][0], op[m][0][1]));
#pragma unroll
        for (int p = 0; p < 3; ++p) st16(wr, obase + m * 1024 + (p + 1) * 4096, pairfrag(op[m][2 * p + 1][0], op[m][2 * p + 1][1], op[m][2 * p + 2][0], op[m][2 * p + 2][1]));
      }
    } else {
#pragma unroll
      for (int m = 0; m < 2; ++m) {
#pragma unroll
        for (int p = 0; p < 3; ++p) st16(wr, obase + m * 1024 + p * 4096, pairfrag(op[m][2 * p][0], op[m][2 * p][1], op[m][2 * p + 1][0], op[m][2 * p + 1][1]));
        st16(wr, lane < 32 ? obase + m * 1024 + 3 * 4096 : 0x80000000u, pairfrag(op[m][6][0], op[m][6][1], op[m][6][0], op[m][6][1]));
      }
    }
    STAMP(2, 3);
    wait_v<0>();
    publish_wg(ws, FL_F3 + (rb * 16 + tf) * 256, ep, lane, (LAS unsigned*)(misc + 1006));
    STAMP(2, 4);
    bar();
  }
  {
    const int mh = w >> 1, kh = w & 1;
    const int T = tb * 4 + w;
    f32x4 acc[2][5];
#pragma unroll
    for (int m = 0; m < 2; ++m)
#pragma unroll
      for (int n = 0; n < 5; ++n) acc[m][n] = (f32x4){0, 0, 0, 0};
    auto rd = [&](int slot, int ks, int set) {
      LAS char* d = smem + slot * ST + (ks * 2 + kh) * 9216 + lane * 16;
#pragma unroll
      for (int m = 0; m < 2; ++m) fa[set][m] = *(LAS bf16x8*)(d + (mh * 2 + m) * 1024);
#pragma unroll
      for (int n = 0; n < 5; ++n) fb[set][n] = *(LAS bf16x8*)(d + 4096 + n * 1024);
    };
    auto cp = [&](int set) {
#pragma unroll
      for (int n = 0; n < 5; ++n)
#pragma unroll
        for (int m = 0; m < 2; ++m) acc[m][n] = mfma(fb[set][n], fa[set][m], acc[m][n]);
    };
    bar();
    STAMP(3, 1);
    u32x2 hres[5];
#pragma unroll
    for (int n = 0; n < 5; ++n) { const int tau = te * 5 + n; hres[n] = ld8(wr, WS_HF + (unsigned)(((T >> 3) * 20 + (tau >> 1)) * 8 + (T & 7)) * 1024 + (((tau & 1) * 2 + (fq >> 1)) * 16 + fr) * 16 + (fq & 1) * 8); }
    consumer_loop<14, 2, 1, 7, 10, false>(1, rd, cp, [](int) {});
    STAMP(3, 2);
    LAS f32x4* red = (LAS f32x4*)smem;
#pragma unroll
    for (int n = 0; n < 5; ++n) red[((w ^ 1) * 5 + n) * 64 + lane] = kh ? acc[0][n] : acc[1][n];
    wait_l0(); bar();
    unsigned op[5][2];
#pragma unroll
    for (int n = 0; n < 5; ++n) {
      f32x4 v = (kh ? acc[1][n] : acc[0][n]) + red[(w * 5 + n) * 64 + lane];
      v[0] += bflo(hres[n].x); v[1] += bfhi(hres[n].x); v[2] += bflo(hres[n].y); v[3] += bfhi(hres[n].y);
      op[n][0] = pk2(v[0], v[1]); op[n][1] = pk2(v[2], v[3]);
    }
    bf16_t* orow = out + (size_t)(T * 16 + fr) * D + te * 80;
    *(u32x4*)(orow + fq * 8) = pairfrag(op[0][0], op[0][1], op[1][0], op[1][1]);
    *(u32x4*)(orow + 32 + fq * 8) = pairfrag(op[2][0], op[2][1], op[3][0], op[3][1]);
    *(u32x2*)(orow + 64 + fq * 4) = (u32x2){op[4][0], op[4][1]};
    if (bid == 0 && tid == 0) __hip_atomic_store((GAS u64*)(ws + FL_E), ep, __ATOMIC_RELAXED, __HIP_MEMORY_SCOPE_AGENT);
    STAMP(3, 3);
  }
}

extern "C" void kernel_launch(void* const* d_in, const int* in_sizes, int n_in, void* d_out, int out_size, void* d_ws, size_t ws_size, hipStream_t stream) {
  (void)hipFuncSetAttribute((const void*)fnet_glu_kernel, hipFuncAttributeMaxDynamicSharedMemorySize, LDS_BYTES);
  fnet_glu_kernel<<<256, 512, LDS_BYTES, stream>>>((const bf16_t*)d_in[0], (const bf16_t*)d_in[1], (const bf16_t*)d_in[2], (const bf16_t*)d_in[3], (const bf16_t*)d_in[4],
                                                   (const bf16_t*)d_in[6], (const bf16_t*)d_in[7], (const bf16_t*)d_in[8], (char*)d_ws, (bf16_t*)d_out);
}
```

```cpp
#include <hip/hip_runtime.h>
#include <hip/hip_bf16.h>
typedef unsigned short bf16_t;
typedef unsigned long long u64;
typedef short bf16x8 __attribute__((ext_vector_type(8)));
typedef float f32x4 __attribute__((ext_vector_type(4)));
typedef float f32x2 __attribute__((ext_vector_type(2)));
typedef unsigned u32x4 __attribute__((ext_vector_type(4)));
typedef unsigned u32x2 __attribute__((ext_vector_type(2)));
typedef __bf16 bf16x2_t __attribute__((ext_vector_type(2)));
#define LAS __attribute__((address_space(3)))
#define GAS __attribute__((address_space(1)))
#define DEV __device__ __forceinline__

constexpr int S = 2048, D = 640, F = 1728;
constexpr unsigned WS_ABF = 0;
constexpr unsigned WS_HF = WS_ABF + 5242880;
constexpr unsigned WS_WF = WS_HF + 2621440;
constexpr unsigned WS_HHF = WS_WF + 4423680;
constexpr unsigned WS_WDF = WS_HHF + 7077888;
constexpr unsigned WS_XB = WS_WDF + 2211840;
constexpr unsigned WS_CS = WS_XB + 7864320;
constexpr unsigned WS_ST = WS_CS + 138240;
constexpr unsigned WS_FL = WS_ST + 131072;
constexpr unsigned FL_E = WS_FL, FL_F1 = WS_FL + 256, FL_F2 = FL_F1 + 65536, FL_F3 = FL_F2 + 65536, FL_FW = FL_F3 + 65536, FL_FX = FL_FW + 8192;
constexpr unsigned WS_S1 = FL_FX + 131072;
constexpr unsigned WS_WS = WS_S1 + 32768;
constexpr unsigned WS_END = WS_WS + 10240;
constexpr int ST = 52 * 1024;
constexpr int LDS_BYTES = 3 * ST + 4096;

template <int AUX> DEV void glds16(const void* g, LAS void* l) { __builtin_amdgcn_global_load_lds((const GAS unsigned*)g, (LAS unsigned*)l, 16, 0, AUX); }
template <int N> DEV void wait_v() { asm volatile("s_waitcnt vmcnt(%0)" ::"i"(N) : "memory"); }
DEV void wait_l0() { asm volatile("s_waitcnt lgkmcnt(0)" ::: "memory"); }
DEV void bar() { asm volatile("" ::: "memory"); __builtin_amdgcn_s_barrier(); asm volatile("" ::: "memory"); }
DEV f32x4 mfma(bf16x8 a, bf16x8 b, f32x4 c) { return __builtin_amdgcn_mfma_f32_16x16x32_bf16(a, b, c, 0, 0, 0); }
DEV unsigned pk2(float lo, float hi) { bf16x2_t v; v.x = (__bf16)lo; v.y = (__bf16)hi; return __builtin_bit_cast(unsigned, v); }
DEV float bflo(unsigned u) { return __uint_as_float(u << 16); }
DEV float bfhi(unsigned u) { return __uint_as_float(u & 0xffff0000u); }
DEV float dot2(unsigned a, unsigned b, float c) { return __builtin_amdgcn_fdot2_f32_bf16(__builtin_bit_cast(bf16x2_t, a), __builtin_bit_cast(bf16x2_t, b), c, false); }
typedef __amdgpu_buffer_rsrc_t rsrc_t;
DEV rsrc_t mkrsrc(const void* p, unsigned bytes) { return __builtin_amdgcn_make_buffer_rsrc((void*)p, 0, bytes, 0x00020000); }
DEV void st16(rsrc_t r, unsigned off, u32x4 v) { __builtin_amdgcn_raw_buffer_store_b128(v, r, off, 0, 16); }
DEV void st8(rsrc_t r, unsigned off, u32x2 v) { __builtin_amdgcn_raw_buffer_store_b64(v, r, off, 0, 16); }
DEV void st4(rsrc_t r, unsigned off, unsigned v) { __builtin_amdgcn_raw_buffer_store_b32(v, r, off, 0, 16); }
DEV u32x4 ld16(rsrc_t r, unsigned off) { return __builtin_amdgcn_raw_buffer_load_b128(r, off, 0, 16); }
DEV u32x2 ld8(rsrc_t r, unsigned off) { return __builtin_amdgcn_raw_buffer_load_b64(r, off, 0, 16); }
DEV unsigned ld4(rsrc_t r, unsigned off) { return __builtin_amdgcn_raw_buffer_load_b32(r, off, 0, 16); }
DEV void publish(char* ws, unsigned off, u64 ep, int lane) {
  if (lane == 0) __hip_atomic_store((GAS u64*)(ws + off), ep, __ATOMIC_RELAXED, __HIP_MEMORY_SCOPE_AGENT);
}
DEV void publish_wg(char* ws, unsigned off, u64 ep, int lane, LAS unsigned* cnt, unsigned last = 3u) {
  unsigned old = 0;
  if (lane == 0) old = __hip_atomic_fetch_add(cnt, 1u, __ATOMIC_RELAXED, __HIP_MEMORY_SCOPE_WORKGROUP);
  if (__builtin_amdgcn_readfirstlane(old) == last) publish(ws, off, ep, lane);
}
DEV void await(char* ws, unsigned off  , u64 ep) {
  GAS u64* p = (GAS u64*)(ws + off);
#pragma unroll 1
  for (int spins = 0; spins < (1 << 16); ++spins) {
    const u64 v = __hip_atomic_load(p, __ATOMIC_RELAXED, __HIP_MEMORY_SCOPE_AGENT);
    if (__all(v == ep)) break;
    __builtin_amdgcn_s_sleep(2);
  }
  __builtin_amdgcn_fence(__ATOMIC_ACQUIRE, "wavefront");
  asm volatile("" ::: "memory");
}
template <int CTRL> DEV float dppadd(float v) { return v + __uint_as_float(__builtin_amdgcn_update_dpp(0, __float_as_uint(v), CTRL, 0xf, 0xf, true)); }
DEV float qsum(float v);
DEV float wsum64(float v) {
  v = dppadd<0xB1>(v); v = dppadd<0x4E>(v); v = dppadd<0x141>(v); v = dppadd<0x140>(v);
  return qsum(v);
}
DEV float qsum(float v) {
  auto a = __builtin_amdgcn_permlane16_swap(__float_as_uint(v), __float_as_uint(v), false, false);
  v = __uint_as_float(a[0]) + __uint_as_float(a[1]);
  auto b = __builtin_amdgcn_permlane32_swap(__float_as_uint(v), __float_as_uint(v), false, false);
  return __uint_as_float(b[0]) + __uint_as_float(b[1]);
}
DEV u32x4 pairfrag(unsigned a0, unsigned a1, unsigned b0, unsigned b1) {
  auto p0 = __builtin_amdgcn_permlane32_swap(a0, b0, false, false);
  auto p1 = __builtin_amdgcn_permlane32_swap(a1, b1, false, false);
  auto q0 = __builtin_amdgcn_permlane16_swap(p0[0], p0[1], false, false);
  auto q1 = __builtin_amdgcn_permlane16_swap(p1[0], p1[1], false, false);
  return (u32x4){q0[0], q1[0], q0[1], q1[1]};
}
#define SB0() __builtin_amdgcn_sched_barrier(0)
DEV int nxt(int slot) { return slot == 2 ? 0 : slot + 1; }
template <int NRD, int NMF> DEV void interleave() {
#pragma unroll
  for (int i = 0; i < NRD; ++i) { __builtin_amdgcn_sched_group_barrier(0x8, 1, 0); __builtin_amdgcn_sched_group_barrier(0x100, 1, 0); }
  __builtin_amdgcn_sched_group_barrier(0x8, NMF - NRD, 0);
}
template <int NS, int KS, int KSL, int NRD, int NMF, bool ENDBAR, class Rd, class Cp, class Mid>
DEV void consumer_loop(int slot, Rd rd, Cp cp, Mid mid) {
  rd(slot, 0, 0);
  SB0();
#pragma unroll 1
  for (int s = 0; s < NS - 1; ++s) {
    const int nslot = nxt(slot);
    mid(s);
#pragma unroll
    for (int ks = 0; ks < KS - 1; ++ks) { rd(slot, ks + 1, (ks + 1) & 1); cp(ks & 1); interleave<NRD, NMF>(); SB0(); }
    wait_l0(); bar();
    rd(nslot, 0, 0); cp(1); interleave<NRD, NMF>(); SB0();
    slot = nslot;
  }
#pragma unroll
  for (int ks = 0; ks < KSL - 1; ++ks) { rd(slot, ks + 1, (ks + 1) & 1); cp(ks & 1); interleave<NRD, NMF>(); SB0(); }
  if (ENDBAR) { wait_l0(); bar(); }
  cp((KSL - 1) & 1);
  SB0();
}

DEV void mf(f32x4& c, const u32x4& a, const u32x4& b) { asm volatile("v_mfma_f32_16x16x32_bf16 %0, %1, %2, %0" : "+v"(c) : "v"(a), "v"(b)); }
template <int OFF> DEV void rdl(u32x4& d, unsigned addr) { asm volatile("ds_read_b128 %0, %1 offset:%2" : "=v"(d) : "v"(addr), "i"(OFF)); }
template <int I, int MT, int NT, int AOFF, int ASTR, int BOFF, int BSTR>
DEV void rd_i(u32x4 (&na)[MT], u32x4 (&nb)[NT], unsigned aaddr, unsigned baddr) {
  if constexpr (I < MT) rdl<AOFF + I * ASTR>(na[I], aaddr);
  else if constexpr (I < MT + NT) rdl<BOFF + (I - MT) * BSTR>(nb[I - MT], baddr);
}
template <int MT, int NT> DEV void frags_ready(u32x4 (&ca)[MT], u32x4 (&cb)[NT]) {
  asm volatile("s_waitcnt lgkmcnt(0)" ::: "memory");
#pragma unroll
  for (int m = 0; m < MT; ++m) asm volatile("" : "+v"(ca[m]));
#pragma unroll
  for (int n = 0; n < NT; ++n) asm volatile("" : "+v"(cb[n]));
}
template <int I, int MT, int NT, bool SWAP, bool BARRIER, bool READ, int AOFF, int ASTR, int BOFF, int BSTR>
DEV void step_i(f32x4 (&acc)[MT][NT], u32x4 (&ca)[MT], u32x4 (&cb)[NT], u32x4 (&na)[MT], u32x4 (&nb)[NT], unsigned aaddr, unsigned baddr) {
  if constexpr (I < MT * NT) {
    if constexpr (SWAP) mf(acc[I % MT][I / MT], cb[I / MT], ca[I % MT]); else mf(acc[I % MT][I / MT], ca[I % MT], cb[I / MT]);
    if constexpr (BARRIER && I == 0) asm volatile("s_barrier" ::: "memory");
    if constexpr (READ) rd_i<I, MT, NT, AOFF, ASTR, BOFF, BSTR>(na, nb, aaddr, baddr);
    step_i<I + 1, MT, NT, SWAP, BARRIER, READ, AOFF, ASTR, BOFF, BSTR>(acc, ca, cb, na, nb, aaddr, baddr);
  }
}
template <int MT, int NT, bool SWAP, bool BARRIER, bool READ, int AOFF, int ASTR, int BOFF, int BSTR>
DEV void step(f32x4 (&acc)[MT][NT], u32x4 (&ca)[MT], u32x4 (&cb)[NT], u32x4 (&na)[MT], u32x4 (&nb)[NT], unsigned aaddr, unsigned baddr) {
  frags_ready<MT, NT>(ca, cb);
  step_i<0, MT, NT, SWAP, BARRIER, READ, AOFF, ASTR, BOFF, BSTR>(acc, ca, cb, na, nb, aaddr, baddr);
}
template <int I, int MT, int NT, int AOFF, int ASTR, int BOFF, int BSTR>
DEV void rd_all(u32x4 (&na)[MT], u32x4 (&nb)[NT], unsigned aaddr, unsigned baddr) {
  if constexpr (I < MT + NT) { rd_i<I, MT, NT, AOFF, ASTR, BOFF, BSTR>(na, nb, aaddr, baddr); rd_all<I + 1, MT, NT, AOFF, ASTR, BOFF, BSTR>(na, nb, aaddr, baddr); }
}
template <int MT, int NT> DEV void acc_done(f32x4 (&acc)[MT][NT]) {
  asm volatile("s_nop 7\n\ts_nop 7" ::: "memory");
#pragma unroll
  for (int m = 0; m < MT; ++m)
#pragma unroll
    for (int n = 0; n < NT; ++n) asm volatile("" : "+v"(acc[m][n]));
}
template <int MT, int NT> DEV void acc_zero(f32x4 (&acc)[MT][NT]) {
#pragma unroll
  for (int m = 0; m < MT; ++m)
#pragma unroll
    for (int n = 0; n < NT; ++n) { acc[m][n] = (f32x4){0, 0, 0, 0}; asm volatile("" : "+v"(acc[m][n])); }
}
template <bool BARRIER, int OFF>
DEV void step3(f32x4 (&acc)[4][7], u32x4 (&ca)[4], u32x4 (&cb)[7], u32x4 (&na)[4], u32x4 (&nb)[7], unsigned aaddr, unsigned baddr, unsigned baddr7) {
  frags_ready<4, 7>(ca, cb);
#pragma unroll
  for (int i = 0; i < 28; ++i) {
    mf(acc[i & 3][i >> 2], cb[i >> 2], ca[i & 3]);
    if (BARRIER && i == 0) asm volatile("s_barrier" ::: "memory");
    if (i == 0) rdl<OFF>(na[0], aaddr); if (i == 1) rdl<OFF + 1024>(na[1], aaddr); if (i == 2) rdl<OFF + 2048>(na[2], aaddr); if (i == 3) rdl<OFF + 3072>(na[3], aaddr);
    if (i == 4) rdl<OFF>(nb[0], baddr); if (i == 5) rdl<OFF + 1024>(nb[1], baddr); if (i == 6) rdl<OFF + 2048>(nb[2], baddr); if (i == 7) rdl<OFF + 3072>(nb[3], baddr);
    if (i == 8) rdl<OFF + 4096>(nb[4], baddr); if (i == 9) rdl<OFF + 5120>(nb[5], baddr); if (i == 10) rdl<OFF>(nb[6], baddr7);
  }
}
DEV void lsig(LAS unsigned* c, int lane) { wait_l0(); if (lane == 0) __hip_atomic_fetch_add(c, 1u, __ATOMIC_RELAXED, __HIP_MEMORY_SCOPE_WORKGROUP); }
DEV void lwait(LAS unsigned* c, unsigned n) {
#pragma unroll 1
  for (int i = 0; i < (1 << 22); ++i) { if (__builtin_amdgcn_readfirstlane(*(volatile LAS unsigned*)c) >= n) break; __builtin_amdgcn_s_sleep(1); }
  asm volatile("" ::: "memory");
}

__global__ __launch_bounds__(512, 2) void fnet_glu_kernel(const bf16_t* __restrict__ x, const bf16_t* __restrict__ Cre, const bf16_t* __restrict__ Cim,
                                                          const bf16_t* __restrict__ Wre, const bf16_t* __restrict__ Wim, const bf16_t* __restrict__ Wg,
                                                          const bf16_t* __restrict__ Wu, const bf16_t* __restrict__ Wd, char* ws, bf16_t* __restrict__ out) {
  extern __shared__ __attribute__((aligned(16))) char smem_[];
  LAS char* smem = (LAS char*)smem_;
  const int tid = threadIdx.x, lane = tid & 63, w8 = __builtin_amdgcn_readfirstlane(tid >> 6), w = w8 & 3;
  const int bid = blockIdx.x, xcd = bid & 7, idx = bid >> 3;
  const int half = xcd >> 2, te = idx & 7, tt = (xcd & 3) * 4 + (idx >> 3), g = half * 8 + te;
  const int q2 = xcd >> 1, t2 = (xcd & 1) * 8 + (idx >> 2), e2 = idx & 3;
  const int rb = (xcd & 1) * 8 + (idx >> 2), tf = (xcd >> 1) * 4 + (idx & 3), tau0 = (tf * 27) >> 2;
  const int tb = xcd * 4 + (idx >> 3);
  const int fr = lane & 15, fq = lane >> 4, r8 = lane >> 3, c8 = lane & 7;
  const int rd0 = fr * 128 + ((fq ^ (fr >> 1)) << 4), rd1 = fr * 128 + (((fq + 4) ^ (fr >> 1)) << 4);
  LAS float* misc = (LAS float*)(smem + 3 * ST);
  const rsrc_t wr = mkrsrc(ws, WS_END);
  u64 ep;
  const bool worker = (idx & 1) == 0;
  if (!worker) {
    LAS char* dummy = smem + 3 * ST + 2048 + w * 256;
    u64 e0 = __hip_atomic_load((GAS u64*)(ws + FL_E), __ATOMIC_RELAXED, __HIP_MEMORY_SCOPE_AGENT);
    const int blk = w8 == 4 ? 1023 : (w8 - (w8 > 4)) * 128 + (bid >> 4) * 8 + xcd;
    const int xr0 = (xcd & 3) * 512 + (half * 128 + (idx >> 1) * 8 + w8) * 2;
    const int wq = ((xcd & 3) * 16 + (idx >> 1)) * 8 + w8;
    u32x4 xa[2], xb[2], wa[2], wb2[2];
    const bool tl = lane < 16;
#pragma unroll
    for (int r = 0; r < 2; ++r) {
      const bf16_t* xr = x + (size_t)(xr0 + r) * D; xa[r] = *(const u32x4*)(xr + lane * 8); xb[r] = *(const u32x4*)(xr + 512 + (lane & 15) * 8);
      int wrow = wq + r * 512; if (wrow >= 640) wrow = wq;
      const bf16_t* wr_ = (half ? Wim : Wre) + (size_t)wrow * D;
      wa[r] = *(const u32x4*)(wr_ + lane * 8); wb2[r] = *(const u32x4*)(wr_ + 512 + (lane & 15) * 8);
    }
    asm volatile("" ::: "memory");
    unsigned tc0, tc1;
    {
      const int hx = (idx >> 1) * 512 + tid;
      const int lx = hx < 5120 ? hx : 5119;
      tc0 = *(const unsigned*)(x + (size_t)((xcd & 3) * 512 + (lx & 511)) * D + (lx >> 9) * 64);
      const int lw = hx < 6400 ? hx : 6399;
      tc1 = *(const unsigned*)((half ? Wim : Wre) + (size_t)(lw % 640) * D + (lw / 640) * 64);
    }
    __builtin_amdgcn_sched_barrier(0);
    const int mat = blk / 270, rem = blk % 270;
    int kb, nb, ldw; const bf16_t* wsrc;
    if (mat < 2) { kb = rem / 27; nb = rem % 27; ldw = F; wsrc = mat ? Wu : Wg; } else { kb = rem / 10; nb = rem % 10; ldw = D; wsrc = Wd; }
    if (blk >= 810) { kb = 0; nb = 0; }
    LAS char* tbuf = smem + (w8 >> 1) * 36864 + 16384 + (w8 & 1) * 8192;
    e0 = e0 * 6364136223846793005ull + 1442695040888963407ull;
    { const unsigned lo = __builtin_amdgcn_readfirstlane((unsigned)e0), hi = __builtin_amdgcn_readfirstlane((unsigned)(e0 >> 32));
      ep = ((u64)hi << 32) | lo;
      if (tid == 0) { *(LAS u32x4*)(misc + 1004) = (u32x4){0, 0, 0, 0}; *(LAS u32x4*)(misc + 1008) = (u32x4){0, 0, 0, 0}; *(LAS u32x4*)(misc + 1012) = (u32x4){0, 0, 0, 0}; } }
    float gv = 0.f;
#pragma unroll
    for (int r = 0; r < 2; ++r) {
      float sm = 0.f, st = 0.f, ww = 0.f, wt = 0.f;
#pragma unroll
      for (int i = 0; i < 4; ++i) { sm = dot2(xa[r][i], 0x3F803F80u, sm); st = dot2(xb[r][i], 0x3F803F80u, st); ww = dot2(wa[r][i], 0x3F803F80u, ww); wt = dot2(wb2[r][i], 0x3F803F80u, wt); }
      const float mu = wsum64(sm + (tl ? st : 0.f)) * (1.0f / D);
      ww = wsum64(ww + (tl ? wt : 0.f));
      float q = 0.f, qt = 0.f;
#pragma unroll
      for (int i = 0; i < 4; ++i) {
        float d0 = bflo(xa[r][i]) - mu, d1 = bfhi(xa[r][i]) - mu; q += d0 * d0 + d1 * d1;
        d0 = bflo(xb[r][i]) - mu; d1 = bfhi(xb[r][i]) - mu; qt += d0 * d0 + d1 * d1;
      }
      const float rstd = __builtin_amdgcn_rsqf(wsum64(q + (tl ? qt : 0.f)) * (1.0f / D) + 1e-5f);
      gv = lane == 2 * r ? mu : lane == 2 * r + 1 ? rstd : lane == 4 + r ? ww : gv;
    }
    {
      const int wrow = wq + (lane - 4) * 512;
      const unsigned go = lane < 4 ? WS_S1 + (unsigned)(xr0 * 2 + lane) * 8 : (lane < 6 && wrow < 640) ? WS_WS + (unsigned)(half * 640 + wrow) * 8 : 0x80000000u;
      st8(wr, go, (u32x2){__float_as_uint(gv), (unsigned)ep});
    }
    asm volatile("" :: "v"(tc0), "v"(tc1));
    wait_l0(); bar();
    auto ldblk = [&]() {
      const bf16_t* src = wsrc + (size_t)(kb * 64 + r8) * ldw + nb * 64 + c8 * 8;
#pragma unroll
      for (int p = 0; p < 8; ++p) glds16<2>(src + (size_t)p * 8 * ldw, tbuf + p * 1024);
    };
    if (blk < 810) ldblk();
    {
      const bf16_t* M = (half ? Cim : Cre) + (q2 & 1) * 1024;
      if (w8 >= 4) {
        const int sE = (c8 ^ (r8 >> 1)) << 3, sO = (c8 ^ (4 + (r8 >> 1))) << 3;
        const bf16_t* ca = M + (size_t)(t2 * 128 + w * 32 + r8) * S;
#pragma unroll
        for (int st = 0; st < 4; ++st)
#pragma unroll
          for (int p = 0; p < 4; ++p) glds16<0>(ca + ((p & 1) ? 8 * S + sO : sE) + (p >> 1) * 16 * S + st * 64, smem + st * 36864 + (w * 4 + p) * 1024);
        wait_v<16>();
      } else {
#pragma unroll
        for (int rep = 0; rep < 3; ++rep) {
          const int row = (xcd & 1) * 1024 + (idx >> 1) * 64 + (tid >> 2);
          __builtin_amdgcn_global_load_lds((const GAS unsigned*)(M + (size_t)row * S + (4 + rep * 4 + (tid & 3)) * 64), (LAS unsigned*)dummy, 4, 0, 0);
        }
        wait_v<3>();
      }
    }
    if (blk < 810) {
    u32x2 lo[4][2], hi[4][2];
    {
      const unsigned tbase = (unsigned)(size_t)tbuf + ((fq * 8 + (fr >> 2)) * 128 + (fr & 3) * 8);
#pragma unroll
      for (int c = 0; c < 4; ++c)
#pragma unroll
        for (int kk = 0; kk < 2; ++kk)
          asm volatile("ds_read_b64_tr_b16 %0, %2 offset:%3\n\tds_read_b64_tr_b16 %1, %2 offset:%4"
                       : "=&v"(lo[c][kk]), "=&v"(hi[c][kk]) : "v"(tbase), "i"(kk * 4096 + c * 32), "i"(kk * 4096 + 512 + c * 32) : "memory");
      asm volatile("s_waitcnt lgkmcnt(0)" : "+v"(lo[0][0]), "+v"(lo[0][1]), "+v"(lo[1][0]), "+v"(lo[1][1]), "+v"(lo[2][0]), "+v"(lo[2][1]), "+v"(lo[3][0]), "+v"(lo[3][1]),
                   "+v"(hi[0][0]), "+v"(hi[0][1]), "+v"(hi[1][0]), "+v"(hi[1][1]), "+v"(hi[2][0]), "+v"(hi[2][1]), "+v"(hi[3][0]), "+v"(hi[3][1]) :: "memory");
    }
#pragma unroll
    for (int c = 0; c < 4; ++c) {
      float cs = 0.f;
#pragma unroll
      for (int kk = 0; kk < 2; ++kk) {
        u32x4 o = {lo[c][kk].x, lo[c][kk].y, hi[c][kk].x, hi[c][kk].y};
        unsigned d2;
        if (mat < 2) d2 = WS_WF + (unsigned)(((kb * 2 + kk) * 108 + nb * 4 + c) * 2 + mat) * 1024;
        else { int eps = nb * 4 + c; d2 = WS_WDF + (unsigned)(((eps / 5) * 54 + kb * 2 + kk) * 5 + eps % 5) * 1024; }
        st16(wr, mat < 3 ? d2 + lane * 16 : 0x80000000u, o);
#pragma unroll
        for (int i = 0; i < 4; ++i) cs = dot2(o[i], 0x3F803F80u, cs);
      }
      cs = qsum(cs);
      st4(wr, (mat < 2 && lane < 16) ? WS_CS + (unsigned)((kb * 2 + mat) * F + nb * 64 + c * 16 + lane) * 4 : 0x80000000u, __float_as_uint(cs));
    }
    }
  }
  if (w8 >= 4) {
    __builtin_amdgcn_s_setprio(3);
    const int sE = (c8 ^ (r8 >> 1)) << 3, sO = (c8 ^ (4 + (r8 >> 1))) << 3;
    const int rowE = 8 * (r8 >> 2) + (r8 & 3);
    const bf16_t* xa = x + (size_t)(tt * 128 + w * 32) * D;
    const bf16_t* A1 = xa + rowE * D + sE;
    const bf16_t* A1o = xa + (rowE + 16) * D + sO;
    const bf16_t* wb = (half ? Wim : Wre) + (size_t)(te * 80 + w * 40 + r8) * D;
    const bf16_t* B1 = wb + ((w & 1) ? sO : sE);
    const bf16_t* B1o = wb + ((w & 1) ? sE : sO);
    auto issue1 = [&](int s) {
      LAS char* d = smem + (s & 3) * 36864;
#pragma unroll
      for (int p = 0; p < 4; ++p) glds16<0>(((p & 1) ? A1o : A1) + (p >> 1) * 4 * D + s * 64, d + (w * 4 + p) * 1024);
#pragma unroll
      for (int p = 0; p < 5; ++p) glds16<0>(((p & 1) ? B1o : B1) + p * 8 * D + s * 64, d + 16384 + (w * 5 + p) * 1024);
    };
    const bf16_t* ca = (half ? Cim : Cre) + (size_t)(t2 * 128 + w * 32 + r8) * S + (q2 & 1) * 1024;
    const bf16_t* A2 = ca + sE;
    const bf16_t* A2o = ca + 8 * S + sO;
    const char* B2 = ws + WS_ABF + (size_t)((half * 4 + e2) * 64 + (q2 & 1) * 32) * 10240 + w * 5120 + lane * 16;
    auto i2 = [&](int s) {
      LAS char* d = smem + (s & 3) * 36864;
#pragma unroll
      for (int p = 0; p < 4; ++p) glds16<0>(((p & 1) ? A2o : A2) + (p >> 1) * 16 * S + s * 64, d + (w * 4 + p) * 1024);
    };
    auto d2 = [&](int s) {
      LAS char* d = smem + (s & 3) * 36864 + 16384;
#pragma unroll
      for (int p = 0; p < 5; ++p) glds16<16>(B2 + (size_t)s * 20480 + p * 1024, d + (w * 5 + p) * 1024);
    };
    const char* W3 = ws + WS_WF + (size_t)(((w >> 1) * 108 + tau0) * 2 + (w & 1) * 7) * 1024 + lane * 16;
    const char* H3 = ws + WS_HF + (size_t)((rb * 20 + (w >> 1)) * 8 + (w & 1) * 4) * 1024 + lane * 16;
    auto i3 = [&](int s, int slot) {
      LAS char* d = smem + slot * ST + (w >> 1) * 22528 + 8192 + (w & 1) * 7168;
#pragma unroll
      for (int p = 0; p < 7; ++p) glds16<16>(W3 + (size_t)s * (2 * 108 * 2048) + p * 1024, d + p * 1024);
    };
    auto d3 = [&](int s, int slot) {
      LAS char* d = smem + slot * ST + (w >> 1) * 22528 + (w & 1) * 4096;
#pragma unroll
      for (int p = 0; p < 4; ++p) glds16<16>(H3 + (size_t)s * 16384 + p * 1024, d + p * 1024);
    };
    const char* W4 = ws + WS_WDF + (size_t)te * 54 * 5120 + lane * 16;
    const char* H4 = ws + WS_HHF + (size_t)tb * 54 * 4096 + lane * 16;
    auto i4 = [&](int s, int slot) {
      int k4 = 4 * s + w; if (k4 > 53) k4 = 53;
      LAS char* d = smem + slot * ST + w * 9216 + 4096;
#pragma unroll
      for (int p = 0; p < 5; ++p) glds16<16>(W4 + (size_t)k4 * 5120 + p * 1024, d + p * 1024);
    };
    auto d4 = [&](int s, int slot) {
      int k4 = 4 * s + w; if (k4 > 53) k4 = 53;
      LAS char* d = smem + slot * ST + w * 9216;
#pragma unroll
      for (int p = 0; p < 4; ++p) glds16<16>(H4 + (size_t)k4 * 4096 + p * 1024, d + p * 1024);
    };

    if (worker) {
      issue1(0); issue1(1); issue1(2);
      wait_v<18>(); bar();
      issue1(3);
#pragma unroll 1
      for (int s = 1; s < 7; ++s) { wait_v<18>(); bar(); issue1(s + 3); }
      { const u32x2 e = *(LAS u32x2*)(misc + 1000); ep = ((u64)__builtin_amdgcn_readfirstlane(e.y) << 32) | (unsigned)__builtin_amdgcn_readfirstlane(e.x); }
      {
        const int t4 = tid & 255;
        const int pos = t4 & 127, rho = pos & 31;
        const int xrow = tt * 128 + (pos & ~31) + 8 * ((rho & 15) >> 2) + 4 * (rho >> 4) + (rho & 3);
        const unsigned o1 = WS_S1 + (unsigned)(xrow * 2 + (t4 >> 7)) * 8, o2 = WS_WS + (unsigned)(g * 80 + (t4 < 160 ? t4 : 0)) * 8;
        u32x2 sv = ld8(wr, o1), wv = ld8(wr, o2);
        wait_v<20>(); bar();
        wait_v<11>(); bar();
#pragma unroll 1
        for (int spins = 0; spins < (1 << 16); ++spins) {
          if (__all(sv.y == (unsigned)ep && wv.y == (unsigned)ep)) break;
          __builtin_amdgcn_s_sleep(4);
          asm volatile("" ::: "memory");
          sv = ld8(wr, o1); wv = ld8(wr, o2);
        }
        misc[256 + pos * 2 + (t4 >> 7)] = (half && t4 >= 128) ? -__uint_as_float(sv.x) : __uint_as_float(sv.x);
        misc[t4] = __uint_as_float(wv.x);
        wait_l0();
      }
      wait_v<0>();
      bar();
      bar();
      lwait((LAS unsigned*)(misc + 1004), 4);
      i2(0); i2(1); i2(2); i2(3);
    }
    if (w == 0) await(ws, FL_F1 + ((half * 4 + e2) * 16 + (q2 & 1) * 8 + (lane & 7)) * 256, ep);
    bar();
    d2(0); d2(1); d2(2); d2(3);
    wait_v<15>(); bar();
    wait_v<10>(); bar();
    d2(4); i2(4); wait_v<14>(); bar();
#pragma unroll 1
    for (int s = 5; s < 16; ++s) { d2(s); i2(s); wait_v<18>(); bar(); }
    wait_v<9>(); bar();
    wait_v<0>(); bar();
    bar();
    lwait((LAS unsigned*)(misc + 1008), 4);
    await(ws, FL_FW + lane * 8, ep); await(ws, FL_FW + 512 + lane * 8, ep);
    i3(0, 0);
    lwait((LAS unsigned*)(misc + 1005), 4);
    i3(1, 1); i3(2, 2);
    if (w == 0) await(ws, FL_F2 + (rb * 16 + fr) * 256, ep);
    bar();
    d3(0, 0); d3(1, 1); d3(2, 2);
    wait_v<8>(); bar();
    wait_v<4>(); bar();
    {
      int slot = 0;
#pragma unroll 1
      for (int s = 3; s < 10; ++s) { d3(s, slot); i3(s, slot); slot = nxt(slot); wait_v<11>(); bar(); }
    }
    {
      const int t4 = tid & 255;
      const int c = t4 < 224 ? t4 : 0;
      const unsigned co = WS_CS + (unsigned)(((c >> 4) & 1) * F + (tau0 + (c >> 5)) * 16 + (c & 15)) * 4;
      const unsigned so = WS_ST + (unsigned)(rb * 128 + (t4 & 127)) * 8;
      unsigned cpart[10]; u32x2 sp[8];
#pragma unroll
      for (int kb = 0; kb < 10; ++kb) cpart[kb] = ld4(wr, co + kb * 2 * F * 4);
#pragma unroll
      for (int i = 0; i < 8; ++i) sp[i] = ld8(wr, so + i * S * 8);
      wait_v<18>(); bar();
      float csum = 0.f;
#pragma unroll
      for (int kb = 0; kb < 10; ++kb) csum += __uint_as_float(cpart[kb]);
      misc[t4] = csum;
      float mean = 0.f, m2 = 0.f;
#pragma unroll
      for (int i = 0; i < 8; ++i) { mean += __uint_as_float(sp[i].x); m2 += __uint_as_float(sp[i].y); }
      mean *= 0.125f;
      float q = 0.f;
#pragma unroll
      for (int i = 0; i < 8; ++i) { const float e = __uint_as_float(sp[i].x) - mean; q += e * e; }
      m2 += 80.0f * q;
      if (t4 < 128) *(LAS f32x2*)(misc + 256 + t4 * 2) = (f32x2){-mean, __builtin_amdgcn_rsqf(m2 * (1.0f / D) + 1e-5f)};
      wait_l0();
    }
    bar();
    i4(0, 1); i4(1, 2); i4(2, 0);
    if (w == 0) await(ws, FL_F3 + ((tb >> 1) * 16 + fr) * 256, ep);
    bar();
    d4(0, 1); d4(1, 2); d4(2, 0);
    wait_v<8>(); bar();
    wait_v<4>(); bar();
    {
      int slot = 1;
#pragma unroll 1
      for (int s = 3; s < 14; ++s) { d4(s, slot); i4(s, slot); slot = nxt(slot); wait_v<9>(); bar(); }
    }
    wait_v<0>(); bar();
    return;
  }

  bf16x8 fa[2][2], fb[2][7];
  if (worker) {
    {
      u64 e0 = __hip_atomic_load((GAS u64*)(ws + FL_E), __ATOMIC_RELAXED, __HIP_MEMORY_SCOPE_AGENT);
      e0 = e0 * 6364136223846793005ull + 1442695040888963407ull;
      const unsigned lo = __builtin_amdgcn_readfirstlane((unsigned)e0), hi = __builtin_amdgcn_readfirstlane((unsigned)(e0 >> 32));
      ep = ((u64)hi << 32) | lo;
      if (tid == 0) { *(LAS u32x2*)(misc + 1000) = (u32x2){lo, hi}; *(LAS u32x4*)(misc + 1004) = (u32x4){0, 0, 0, 0}; *(LAS u32x4*)(misc + 1008) = (u32x4){0, 0, 0, 0}; *(LAS u32x4*)(misc + 1012) = (u32x4){0, 0, 0, 0}; }
      wait_l0();
    }
    f32x4 acc[2][10];
    acc_zero<2, 10>(acc);
    u32x4 a0[2], b0[10], a1[2], b1[10];
    const unsigned lb = (unsigned)(size_t)smem;
    const unsigned aE = lb + w * 4096 + rd0, aO = lb + w * 4096 + rd1, bE = lb + 16384 + rd0, bO = lb + 16384 + rd1;
    bar();
    rd_all<0, 2, 10, 0, 2048, 0, 2048>(a0, b0, aE, bE);
#pragma unroll 1
    for (int s = 0; s < 10; ++s) {
      const unsigned o = (s & 3) * 36864, on = ((s + 1) & 3) * 36864;
      step<2, 10, false, false, true, 0, 2048, 0, 2048>(acc, a0, b0, a1, b1, aO + o, bO + o);
      step<2, 10, false, true, true, 0, 2048, 0, 2048>(acc, a1, b1, a0, b0, aE + on, bE + on);
    }
    frags_ready<2, 10>(a0, b0);
    acc_done<2, 10>(acc);
    float mean[2][4], rs[2][4];
#pragma unroll
    for (int m = 0; m < 2; ++m) {
      const f32x4 s0 = *(LAS f32x4*)(misc + 256 + (w * 32 + m * 16 + fq * 4) * 2), s1 = *(LAS f32x4*)(misc + 256 + (w * 32 + m * 16 + fq * 4) * 2 + 4);
      mean[m][0] = s0[0]; rs[m][0] = s0[1]; mean[m][1] = s0[2]; rs[m][1] = s0[3]; mean[m][2] = s1[0]; rs[m][2] = s1[1]; mean[m][3] = s1[2]; rs[m][3] = s1[3];
    }
    const unsigned dst = WS_ABF + (unsigned)(((g >> 1) * 64 + tt * 4 + w) * 10) * 1024 + lane * 16;
#pragma unroll
    for (int n = 0; n < 10; ++n) {
      const int c = n * 16 + fr;
      const float wsm = misc[c];
      float v[2][4];
#pragma unroll
      for (int m = 0; m < 2; ++m)
#pragma unroll
        for (int j = 0; j < 4; ++j) v[m][j] = rs[m][j] * (acc[m][n][j] - mean[m][j] * wsm);
      u32x4 o; o.x = pk2(v[0][0], v[0][1]); o.y = pk2(v[0][2], v[0][3]); o.z = pk2(v[1][0], v[1][1]); o.w = pk2(v[1][2], v[1][3]);
      st16(wr, dst + n * 1024, o);
    }
    wait_v<0>();
    publish_wg(ws, FL_F1 + ((g >> 1) * 16 + tt) * 256, ep, lane, (LAS unsigned*)(misc + 1004));
  }
  bar();
  {
    const int mh = w >> 1, nh = w & 1, tee = e2 * 2 + nh, mt = mh * 4 + q2;
    f32x4 acc[4][5];
    acc_zero<4, 5>(acc);
    const int trow = t2 * 128 + mt * 16 + fr;
    u32x2 xv[5];
#pragma unroll
    for (int n = 0; n < 5; ++n) xv[n] = *(const u32x2*)(x + (size_t)trow * D + tee * 80 + n * 16 + fq * 4);
    u32x4 a0[4], b0[5], a1[4], b1[5];
    const unsigned lb = (unsigned)(size_t)smem;
    const unsigned aE = lb + mh * 8192 + rd0, aO = lb + mh * 8192 + rd1, bb = lb + 16384 + nh * 5120 + lane * 16;
    const unsigned aE2 = aE + 73728, aO2 = aO + 73728, bb2 = bb + 73728;
    bar();
    rd_all<0, 4, 5, 0, 2048, 0, 1024>(a0, b0, aE, bb);
#pragma unroll 1
    for (int it = 0; it < 4; ++it) {
      if (it == 1 && !worker) {
        wait_v<0>();
        publish_wg(ws, FL_FW + ((bid >> 4) * 8 + xcd) * 8, ep, lane, (LAS unsigned*)(misc + 1007));
      }
      step<4, 5, true, false, true, 0, 2048, 10240, 1024>(acc, a0, b0, a1, b1, aO, bb);
      step<4, 5, true, true, true, 36864, 2048, 36864, 1024>(acc, a1, b1, a0, b0, aE, bb);
      step<4, 5, true, false, true, 36864, 2048, 47104, 1024>(acc, a0, b0, a1, b1, aO, bb);
      step<4, 5, true, true, true, 0, 2048, 0, 1024>(acc, a1, b1, a0, b0, aE2, bb2);
      step<4, 5, true, false, true, 0, 2048, 10240, 1024>(acc, a0, b0, a1, b1, aO2, bb2);
      step<4, 5, true, true, true, 36864, 2048, 36864, 1024>(acc, a1, b1, a0, b0, aE2, bb2);
      step<4, 5, true, false, true, 36864, 2048, 47104, 1024>(acc, a0, b0, a1, b1, aO2, bb2);
      step<4, 5, true, true, true, 0, 2048, 0, 1024>(acc, a1, b1, a0, b0, aE, bb);
    }
    frags_ready<4, 5>(a0, b0);
    acc_done<4, 5>(acc);
    const int tile = t2 * 4 + e2;
    f32x4 kp[5];
#pragma unroll
    for (int j = 0; j < 4; ++j) {
      if (j != q2) {
        const unsigned off = WS_XB + (unsigned)(((tile * 4 + j) * 4 + w) * 3 + ((q2 - j - 1) & 3)) * 2560;
        st16(wr, off + lane * 16, (u32x4){pk2(acc[j][0][0], acc[j][0][1]), pk2(acc[j][0][2], acc[j][0][3]), pk2(acc[j][1][0], acc[j][1][1]), pk2(acc[j][1][2], acc[j][1][3])});
        st16(wr, off + 1024 + lane * 16, (u32x4){pk2(acc[j][2][0], acc[j][2][1]), pk2(acc[j][2][2], acc[j][2][3]), pk2(acc[j][3][0], acc[j][3][1]), pk2(acc[j][3][2], acc[j][3][3])});
        st8(wr, off + 2048 + lane * 8, (u32x2){pk2(acc[j][4][0], acc[j][4][1]), pk2(acc[j][4][2], acc[j][4][3])});
      } else {
#pragma unroll
        for (int n = 0; n < 5; ++n) kp[n] = acc[j][n];
      }
    }
#pragma unroll
    for (int n = 0; n < 5; ++n) { kp[n][0] += bflo(xv[n].x); kp[n][1] += bfhi(xv[n].x); kp[n][2] += bflo(xv[n].y); kp[n][3] += bfhi(xv[n].y); }
    wait_v<0>();
    publish(ws, FL_FX + ((tile * 4 + q2) * 4 + w) * 128, ep, lane);
    lsig((LAS unsigned*)(misc + 1008), lane);
    await(ws, FL_FX + ((tile * 4 + (lane & 3)) * 4 + w) * 128, ep);
    float hv[5][4]; float sum = 0.f;
    unsigned hp[5][2];
    const unsigned off = WS_XB + (unsigned)((tile * 4 + q2) * 4 + w) * 7680;
    unsigned pv[3][10];
#pragma unroll
    for (int i = 0; i < 3; ++i) {
      const u32x4 p0 = ld16(wr, off + i * 2560 + lane * 16), p1 = ld16(wr, off + i * 2560 + 1024 + lane * 16);
      const u32x2 p2 = ld8(wr, off + i * 2560 + 2048 + lane * 8);
      pv[i][0] = p0.x; pv[i][1] = p0.y; pv[i][2] = p0.z; pv[i][3] = p0.w; pv[i][4] = p1.x; pv[i][5] = p1.y; pv[i][6] = p1.z; pv[i][7] = p1.w; pv[i][8] = p2.x; pv[i][9] = p2.y;
    }
#pragma unroll
    for (int n = 0; n < 5; ++n) {
      f32x4 v = kp[n];
      v[0] += (bflo(pv[0][2 * n]) + bflo(pv[1][2 * n])) + bflo(pv[2][2 * n]); v[1] += (bfhi(pv[0][2 * n]) + bfhi(pv[1][2 * n])) + bfhi(pv[2][2 * n]);
      v[2] += (bflo(pv[0][2 * n + 1]) + bflo(pv[1][2 * n + 1])) + bflo(pv[2][2 * n + 1]); v[3] += (bfhi(pv[0][2 * n + 1]) + bfhi(pv[1][2 * n + 1])) + bfhi(pv[2][2 * n + 1]);
      hp[n][0] = pk2(v[0], v[1]); hp[n][1] = pk2(v[2], v[3]);
      hv[n][0] = bflo(hp[n][0]); hv[n][1] = bfhi(hp[n][0]); hv[n][2] = bflo(hp[n][1]); hv[n][3] = bfhi(hp[n][1]);
      sum += (hv[n][0] + hv[n][1]) + (hv[n][2] + hv[n][3]);
    }
    const unsigned hb = WS_HF + (unsigned)((t2 * 20 + ((tee * 5) >> 1)) * 8 + mt) * 1024 + lane * 16;
    if (nh) {
      st16(wr, lane >= 32 ? hb : 0x80000000u, pairfrag(hp[0][0], hp[0][1], hp[0][0], hp[0][1]));
      st16(wr, hb + 8192, pairfrag(hp[1][0], hp[1][1], hp[2][0], hp[2][1]));
      st16(wr, hb + 16384, pairfrag(hp[3][0], hp[3][1], hp[4][0], hp[4][1]));
    } else {
      st16(wr, hb, pairfrag(hp[0][0], hp[0][1], hp[1][0], hp[1][1]));
      st16(wr, hb + 8192, pairfrag(hp[2][0], hp[2][1], hp[3][0], hp[3][1]));
      st16(wr, lane < 32 ? hb + 16384 : 0x80000000u, pairfrag(hp[4][0], hp[4][1], hp[4][0], hp[4][1]));
    }
    const float mu = qsum(sum) * (1.0f / 80.0f);
    float m2 = 0.f;
#pragma unroll
    for (int n = 0; n < 5; ++n)
#pragma unroll
      for (int j = 0; j < 4; ++j) { float d = hv[n][j] - mu; m2 += d * d; }
    m2 = qsum(m2);
    if (lane < 16) st8(wr, WS_ST + (unsigned)(tee * S + trow) * 8, (u32x2){__float_as_uint(mu), __float_as_uint(m2)});
    wait_v<0>();
    publish_wg(ws, FL_F2 + (t2 * 16 + q2 * 4 + e2) * 256, ep, lane, (LAS unsigned*)(misc + 1005));
    bar();
  }
  {
    const int mh = w >> 1, nh = w & 1;
    f32x4 acc[4][7];
    acc_zero<4, 7>(acc);
    u32x4 a0[4], b0[7], a1[4], b1[7];
    const unsigned lb = (unsigned)(size_t)smem + lane * 16;
    const unsigned ab = lb + mh * 4096, bb = lb + 8192 + nh * 8192, b7 = lb + 8192 + 6144 + nh * 1024;
    bar();
    rdl<0>(a0[0], ab); rdl<1024>(a0[1], ab); rdl<2048>(a0[2], ab); rdl<3072>(a0[3], ab);
    rdl<0>(b0[0], bb); rdl<1024>(b0[1], bb); rdl<2048>(b0[2], bb); rdl<3072>(b0[3], bb); rdl<4096>(b0[4], bb); rdl<5120>(b0[5], bb); rdl<0>(b0[6], b7);
    {
      const unsigned ab1 = ab + ST, bb1 = bb + ST, b71 = b7 + ST, ab2 = ab + 2 * ST, bb2 = bb + 2 * ST, b72 = b7 + 2 * ST;
#pragma unroll 1
      for (int it = 0; it < 3; ++it) {
        step3<false, 22528>(acc, a0, b0, a1, b1, ab, bb, b7);    step3<true, 0>(acc, a1, b1, a0, b0, ab1, bb1, b71);
        step3<false, 22528>(acc, a0, b0, a1, b1, ab1, bb1, b71); step3<true, 0>(acc, a1, b1, a0, b0, ab2, bb2, b72);
        step3<false, 22528>(acc, a0, b0, a1, b1, ab2, bb2, b72); step3<true, 0>(acc, a1, b1, a0, b0, ab, bb, b7);
      }
      step3<false, 22528>(acc, a0, b0, a1, b1, ab, bb, b7);      step3<true, 0>(acc, a1, b1, a0, b0, ab, bb, b7);
    }
    frags_ready<4, 7>(a0, b0);
    acc_done<4, 7>(acc);
    LAS f32x4* xch = (LAS f32x4*)(smem + 45056);
    xch[((w ^ 1) * 2 + 0) * 64 + lane] = nh ? acc[0][6] : acc[2][6];
    xch[((w ^ 1) * 2 + 1) * 64 + lane] = nh ? acc[1][6] : acc[3][6];
    lsig((LAS unsigned*)(misc + 1012 + (w ^ 1)), lane);
    float nmu[4], rr[4], rg[4];
#pragma unroll
    for (int m = 0; m < 4; ++m) { const f32x2 v = *(LAS f32x2*)(misc + 256 + (mh * 64 + m * 16 + fr) * 2); nmu[m] = v[0]; rr[m] = v[1]; rg[m] = v[1] * -1.44269504f; }
    f32x4 cg[4], cu[4];
#pragma unroll
    for (int t = 0; t < 4; ++t) { const int tl = t < 3 ? nh * 4 + t : 3; cg[t] = *(LAS f32x4*)(misc + tl * 32 + fq * 4); cu[t] = *(LAS f32x4*)(misc + tl * 32 + 16 + fq * 4); }
    const unsigned ob2 = WS_HHF + (unsigned)((rb * 2 + mh) * 54 * 4 + (lane >> 5)) * 1024 + (lane & 31) * 16;
    auto batch = [&](f32x4 (&G)[4], f32x4 (&U)[4], const f32x4 (&CG)[4], const f32x4 (&CU)[4], const float (&NM)[4], const float (&RR)[4], const float (&RG)[4], int n, unsigned off0, unsigned off1) {
#pragma unroll
      for (int i = 0; i < 4; ++i) if (i < n) { G[i] = (G[i] + CG[i] * NM[i]) * RG[i]; }
      SB0();
#pragma unroll
      for (int i = 0; i < 4; ++i) if (i < n) {
#pragma unroll
        for (int j = 0; j < 4; ++j) G[i][j] = __builtin_amdgcn_exp2f(G[i][j]);
        U[i] = (U[i] + CU[i] * NM[i]) * RR[i];
      }
      SB0();
#pragma unroll
      for (int i = 0; i < 4; ++i) if (i < n) G[i] = G[i] + 1.0f;
      SB0();
#pragma unroll
      for (int i = 0; i < 4; ++i) if (i < n) {
#pragma unroll
        for (int j = 0; j < 4; ++j) G[i][j] = __builtin_amdgcn_rcpf(G[i][j]);
      }
      SB0();
      unsigned op[4][2];
#pragma unroll
      for (int i = 0; i < 4; ++i) if (i < n) { const f32x4 o = U[i] * G[i]; op[i][0] = pk2(o[0], o[1]); op[i][1] = pk2(o[2], o[3]); }
      st16(wr, off0, pairfrag(op[0][0], op[0][1], op[1][0], op[1][1]));
      if (n == 4) st16(wr, off1, pairfrag(op[2][0], op[2][1], op[3][0], op[3][1]));
    };
#pragma unroll
    for (int t = 0; t < 3; ++t) {
      const int tau = tau0 + nh * 4 + t;
      const unsigned off = ob2 + (unsigned)((tau >> 1) * 4096 + (tau & 1) * 512);
      f32x4 G[4] = {acc[0][2 * t], acc[1][2 * t], acc[2][2 * t], acc[3][2 * t]}, U[4] = {acc[0][2 * t + 1], acc[1][2 * t + 1], acc[2][2 * t + 1], acc[3][2 * t + 1]};
      const f32x4 CG[4] = {cg[t], cg[t], cg[t], cg[t]}, CU[4] = {cu[t], cu[t], cu[t], cu[t]};
      batch(G, U, CG, CU, nmu, rr, rg, 4, off, off + 2048);
    }
    {
      lwait((LAS unsigned*)(misc + 1012 + w), 1);
      const f32x4 x0 = xch[(w * 2 + 0) * 64 + lane], x1 = xch[(w * 2 + 1) * 64 + lane];
      const f32x4 m0 = nh ? acc[2][6] : acc[0][6], m1 = nh ? acc[3][6] : acc[1][6];
      f32x4 G[4] = {nh ? x0 : m0, nh ? x1 : m1, m0, m0}, U[4] = {nh ? m0 : x0, nh ? m1 : x1, m0, m0};
      const f32x4 CG[4] = {cg[3], cg[3], cg[3], cg[3]}, CU[4] = {cu[3], cu[3], cu[3], cu[3]};
      const float NM[4] = {nh ? nmu[2] : nmu[0], nh ? nmu[3] : nmu[1], 0.f, 0.f}, RR[4] = {nh ? rr[2] : rr[0], nh ? rr[3] : rr[1], 0.f, 0.f}, RG[4] = {nh ? rg[2] : rg[0], nh ? rg[3] : rg[1], 0.f, 0.f};
      const int tau = tau0 + 3;
      batch(G, U, CG, CU, NM, RR, RG, 2, ob2 + (unsigned)((tau >> 1) * 4096 + (tau & 1) * 512 + nh * 2048), 0);
    }
    wait_v<0>();
    publish_wg(ws, FL_F3 + (rb * 16 + tf) * 256, ep, lane, (LAS unsigned*)(misc + 1006));
    bar();
  }
  {
    const int mh = w >> 1, kh = w & 1;
    const int T = tb * 4 + w;
    f32x4 acc[2][5];
#pragma unroll
    for (int m = 0; m < 2; ++m)
#pragma unroll
      for (int n = 0; n < 5; ++n) acc[m][n] = (f32x4){0, 0, 0, 0};
    auto rd = [&](int slot, int ks, int set) {
      LAS char* d = smem + slot * ST + (ks * 2 + kh) * 9216 + lane * 16;
#pragma unroll
      for (int m = 0; m < 2; ++m) fa[set][m] = *(LAS bf16x8*)(d + (mh * 2 + m) * 1024);
#pragma unroll
      for (int n = 0; n < 5; ++n) fb[set][n] = *(LAS bf16x8*)(d + 4096 + n * 1024);
    };
    auto cp = [&](int set) {
#pragma unroll
      for (int n = 0; n < 5; ++n)
#pragma unroll
        for (int m = 0; m < 2; ++m) acc[m][n] = mfma(fb[set][n], fa[set][m], acc[m][n]);
    };
    bar();
    u32x2 hres[5];
#pragma unroll
    for (int n = 0; n < 5; ++n) { const int tau = te * 5 + n; hres[n] = ld8(wr, WS_HF + (unsigned)(((T >> 3) * 20 + (tau >> 1)) * 8 + (T & 7)) * 1024 + (((tau & 1) * 2 + (fq >> 1)) * 16 + fr) * 16 + (fq & 1) * 8); }
    consumer_loop<14, 2, 1, 7, 10, false>(1, rd, cp, [](int) {});
    LAS f32x4* red = (LAS f32x4*)smem;
#pragma unroll
    for (int n = 0; n < 5; ++n) red[((w ^ 1) * 5 + n) * 64 + lane] = kh ? acc[0][n] : acc[1][n];
    wait_l0(); bar();
    unsigned op[5][2];
#pragma unroll
    for (int n = 0; n < 5; ++n) {
      f32x4 v = (kh ? acc[1][n] : acc[0][n]) + red[(w * 5 + n) * 64 + lane];
      v[0] += bflo(hres[n].x); v[1] += bfhi(hres[n].x); v[2] += bflo(hres[n].y); v[3] += bfhi(hres[n].y);
      op[n][0] = pk2(v[0], v[1]); op[n][1] = pk2(v[2], v[3]);
    }
    bf16_t* orow = out + (size_t)(T * 16 + fr) * D + te * 80;
    *(u32x4*)(orow + fq * 8) = pairfrag(op[0][0], op[0][1], op[1][0], op[1][1]);
    *(u32x4*)(orow + 32 + fq * 8) = pairfrag(op[2][0], op[2][1], op[3][0], op[3][1]);
    *(u32x2*)(orow + 64 + fq * 4) = (u32x2){op[4][0], op[4][1]};
    if (bid == 0 && tid == 0) __hip_atomic_store((GAS u64*)(ws + FL_E), ep, __ATOMIC_RELAXED, __HIP_MEMORY_SCOPE_AGENT);
  }
}

extern "C" void kernel_launch(void* const* d_in, const int* in_sizes, int n_in, void* d_out, int out_size, void* d_ws, size_t ws_size, hipStream_t stream) {
  (void)hipFuncSetAttribute((const void*)fnet_glu_kernel, hipFuncAttributeMaxDynamicSharedMemorySize, LDS_BYTES);
  fnet_glu_kernel<<<256, 512, LDS_BYTES, stream>>>((const bf16_t*)d_in[0], (const bf16_t*)d_in[1], (const bf16_t*)d_in[2], (const bf16_t*)d_in[3], (const bf16_t*)d_in[4],
                                                   (const bf16_t*)d_in[6], (const bf16_t*)d_in[7], (const bf16_t*)d_in[8], (char*)d_ws, (bf16_t*)d_out);
}
```
